```python
import torch

def _softmax(x, axis=-1):
    return torch.softmax(x, dim=axis)
def _gelu(x):
    return torch.nn.functional.gelu(x, approximate="tanh")
def _silu(x):
    return torch.nn.functional.silu(x)
def _sigmoid(x):
    return torch.sigmoid(x)
def _layernorm(x, axis=-1, eps=1e-5):
    m = x.mean(dim=axis, keepdim=True)
    v = x.var(dim=axis, keepdim=True, unbiased=False)
    return (x - m) / torch.sqrt(v + eps)
def _scatter_add(tgt, idx, src, axis=0):
    return tgt.clone().index_add_(axis, idx, src)
def _segment_sum(src, seg, n):
    out = torch.zeros((n,) + src.shape[1:], dtype=src.dtype)
    return out.index_add_(0, seg, src)
def _pad(x, pw):
    flat = tuple(v for pair in reversed(pw) for v in pair)
    return torch.nn.functional.pad(x, flat)

kernel_name = 'parallel_sum__sliding_block__moe_arch_mod__prompt'

def setup_inputs():

    _w = (lambda a: (lambda t: t.to(torch.bfloat16) if t.is_floating_point() else t)(torch.as_tensor(a)))
    def _A_setup():
        B, S, D = 1, 2048, 512
        return {
            "x": _w((lambda *s: torch.randn(*s, dtype=torch.bfloat16) * 0.05)(B, S, D)),
            "wqkv": _w((lambda *s: torch.randn(*s, dtype=torch.bfloat16) * 0.05)(D, 3 * D)),
            "wo": _w((lambda *s: torch.randn(*s, dtype=torch.bfloat16) * 0.05)(D, D)),
        }
    _A_in = _A_setup()

    def _B_setup():
        B, S, D, F, C = 1, 2048, 512, 1376, 1
        return {
            "x": _w((lambda *s: torch.randn(*s, dtype=torch.bfloat16) * 0.05)(B, S, D)),
            "w_r": _w((lambda *s: torch.randn(*s, dtype=torch.bfloat16) * 0.05)(D)),
            "w_gate": _w((lambda *s: torch.randn(*s, dtype=torch.bfloat16) * 0.05)(D, F)),
            "w_up": _w((lambda *s: torch.randn(*s, dtype=torch.bfloat16) * 0.05)(D, F)),
            "w_down": _w((lambda *s: torch.randn(*s, dtype=torch.bfloat16) * 0.05)(F, D)),
        }
    _B_in = _B_setup()

    return {
        "A_x": _A_in["x"],
        "A_wqkv": _A_in["wqkv"],
        "A_wo": _A_in["wo"],
        "B_x": _B_in["x"],
        "B_w_r": _B_in["w_r"],
        "B_w_gate": _B_in["w_gate"],
        "B_w_up": _B_in["w_up"],
        "B_w_down": _B_in["w_down"],
    }

def reference(A_x, A_wqkv, A_wo, B_x, B_w_r, B_w_gate, B_w_up, B_w_down):
    def _A_ref(x, wqkv, wo):
        B, S, D = x.shape
        H = 32; Dh = D // H; W = 256
        qkv = (lambda x, *d: x.permute(*d))((x @ wqkv).reshape(B, S, 3, H, Dh), 2, 0, 3, 1, 4)
        q, k, v = qkv[0], qkv[1], qkv[2]
        scores = torch.einsum("bhqd,bhkd->bhqk", q, k) * (Dh ** -0.5)
        idx = torch.arange(S)
        mask = (idx[:, None] - idx[None, :] >= 0) & (idx[:, None] - idx[None, :] < W)
        scores = torch.where(mask[None, None], scores, -1e9)
        p = _softmax(scores, axis=-1)
        o = torch.einsum("bhqk,bhkd->bhqd", p, v)
        return (lambda x, *d: x.permute(*d))(o, 0, 2, 1, 3).reshape(B, S, D) @ wo

    def _B_ref(x, w_r, w_gate, w_up, w_down):
        B, S, D = x.shape
        C = 1
        r = x @ w_r
        idx = torch.argsort(-r, axis=-1)[:, :C]
        idx_e = (lambda x, n, axis: torch.repeat_interleave(x, n, dim=axis))(idx.reshape(B, C, 1), D, axis=2)
        x_sel = (lambda x, i, axis: torch.take_along_dim(x, i, dim=axis))(x, idx_e, axis=1)
        g = _silu(x_sel @ w_gate)
        u = x_sel @ w_up
        y = (g * u) @ w_down
        r_sel = _sigmoid((lambda x, i, axis: torch.take_along_dim(x, i, dim=axis))(r, idx, axis=1))
        y = y * r_sel.reshape(B, C, 1)
        b_off = (torch.arange(B) * S).reshape(B, 1)
        flat_idx = (idx + b_off).reshape(B * C)
        zero = (x * 0.0).reshape(B * S, D)
        upd = _scatter_add(zero, flat_idx, y.reshape(B * C, D), axis=0)
        return x + upd.reshape(B, S, D)

    x = A_x
    xn = _layernorm(x, axis=-1)
    return x + _A_ref(xn, A_wqkv, A_wo) + _B_ref(xn, B_w_r, B_w_gate, B_w_up, B_w_down)
```

```cpp
#include <hip/hip_runtime.h>
#include <hip/hip_bf16.h>
typedef unsigned short u16;
typedef __attribute__((ext_vector_type(8))) short bf16x8;
typedef __attribute__((ext_vector_type(4))) float f32x4;
__device__ __forceinline__ float bf2f(u16 v){ return __uint_as_float(((unsigned)v)<<16); }
__device__ __forceinline__ u16 f2bf(float f){ unsigned u=__float_as_uint(f); u += 0x7fffu + ((u>>16)&1u); return (u16)(u>>16); }
__device__ __forceinline__ float rbf(float f){ return bf2f(f2bf(f)); }
constexpr int S=2048, D=512, F=1376;
__global__ void __launch_bounds__(256) k_ln(const u16* x, const u16* wr, u16* xn, float* r){
  int row = blockIdx.x*4 + (threadIdx.x>>6); int lane = threadIdx.x&63;
  float f[8]; float s=0;
  for(int i=0;i<8;i++){ f[i]=bf2f(x[row*512+lane*8+i]); s+=f[i]; }
  for(int m=32;m>=1;m>>=1) s+=__shfl_xor(s,m);
  float mean=s/512.f; float q=0; for(int i=0;i<8;i++){ float d=f[i]-mean; q+=d*d; }
  for(int m=32;m>=1;m>>=1) q+=__shfl_xor(q,m);
  float mb = rbf(mean), sd = rbf(sqrtf(rbf(rbf(q/512.f)+1e-5f)));
  float rr=0;
  for(int i=0;i<8;i++){ float v = rbf(rbf(f[i]-mb)/sd); xn[row*512+lane*8+i]=f2bf(v); rr += v*bf2f(wr[lane*8+i]); }
  for(int m=32;m>=1;m>>=1) rr+=__shfl_xor(rr,m);
  if(lane==0) r[row]=rbf(rr);
}
template<int MODE>
__global__ void __launch_bounds__(256) k_gemm(const u16* A, const u16* B, int N, u16* C, const u16* x, const u16* xn, const float* y, const int* idxp){
  int w = blockIdx.x*4 + (threadIdx.x>>6), lane = threadIdx.x&63;
  int nt = N/64; int m0 = (w/nt)*16, n0 = (w%nt)*64;
  f32x4 acc[4]; for(int j=0;j<4;j++) acc[j]=f32x4{0,0,0,0};
  for(int k0=0;k0<512;k0+=32){
    bf16x8 a = *(const bf16x8*)(A + (m0+(lane&15))*512 + k0 + (lane>>4)*8);
    for(int j=0;j<4;j++){ bf16x8 b; for(int e=0;e<8;e++) b[e]=(short)B[(k0+(lane>>4)*8+e)*N + n0 + j*16 + (lane&15)];
      acc[j] = __builtin_amdgcn_mfma_f32_16x16x32_bf16(a,b,acc[j],0,0,0); }
  }
  int idx = MODE? *idxp : 0;
  for(int j=0;j<4;j++) for(int e=0;e<4;e++){ int row=m0+(lane>>4)*4+e, col=n0+j*16+(lane&15);
    if(MODE==0) C[row*N+col]=f2bf(acc[j][e]);
    else { float a_=rbf(acc[j][e]); float t=rbf(bf2f(x[row*512+col])+a_); float b_=bf2f(xn[row*512+col]); if(row==idx) b_=rbf(b_+y[col]); C[row*N+col]=f2bf(t+b_);} }
}
__global__ void __launch_bounds__(64) k_attn(const u16* qkv, u16* o){
  int h = blockIdx.x & 31, q = (blockIdx.x>>5)*64 + threadIdx.x;
  float qv[16]; for(int d=0;d<16;d++) qv[d]=bf2f(qkv[q*1536+h*16+d]);
  int j0 = q-255; if(j0<0) j0=0;
  float m=-1e30f;
  for(int j=j0;j<=q;j++){ float s=0; for(int d=0;d<16;d++) s+=qv[d]*bf2f(qkv[j*1536+512+h*16+d]); m=fmaxf(m,s*0.25f);}
  float l=0, acc[16]; for(int d=0;d<16;d++) acc[d]=0;
  for(int j=j0;j<=q;j++){ float s=0; for(int d=0;d<16;d++) s+=qv[d]*bf2f(qkv[j*1536+512+h*16+d]); float p=__expf(s*0.25f-m); l+=p; for(int d=0;d<16;d++) acc[d]+=p*bf2f(qkv[j*1536+1024+h*16+d]); }
  for(int d=0;d<16;d++) o[q*512+h*16+d]=f2bf(acc[d]/l);
}
__global__ void __launch_bounds__(256) k_moe1(const float* r, const u16* xn, const u16* wg, const u16* wu, float* h, int* idxp){
  __shared__ float sv[256]; __shared__ int si[256];
  int t=threadIdx.x; float bv=-1e30f; int bi=0; for(int i=t;i<S;i+=256){ if(r[i]>bv){bv=r[i];bi=i;} }
  sv[t]=bv; si[t]=bi; __syncthreads();
  for(int s=128;s>=1;s>>=1){ if(t<s){ if(sv[t+s]>sv[t] || (sv[t+s]==sv[t] && si[t+s]<si[t])){sv[t]=sv[t+s];si[t]=si[t+s];} } __syncthreads(); }
  int idx=si[0]; if(blockIdx.x==0&&t==0) *idxp=idx;
  int f=blockIdx.x*256+t; if(f<F){ float g=0,u=0; for(int k=0;k<512;k++){ float xv=bf2f(xn[idx*512+k]); g+=xv*bf2f(wg[k*F+f]); u+=xv*bf2f(wu[k*F+f]); }
    g=rbf(g); u=rbf(u); g=rbf(g/(1.f+__expf(-g))); h[f]=rbf(g*u);} }
__global__ void __launch_bounds__(64) k_moe2(const float* r, const float* h, const u16* wd, const int* idxp, float* y){
  int c=blockIdx.x*64+threadIdx.x; float a=0; for(int f=0;f<F;f++) a+=h[f]*bf2f(wd[f*512+c]);
  float rs=rbf(1.f/(1.f+__expf(-r[*idxp]))); y[c]=rbf(rbf(a)*rs); }
#define EXTRA 0
__global__ void k_empty(int* p){ if (p==nullptr) *p=0; }
extern "C" void kernel_launch(void* const* d_in, const int* in_sizes, int n_in, void* d_out, int out_size, void* d_ws, size_t ws_size, hipStream_t stream){
  const u16* x=(const u16*)d_in[0]; const u16* wqkv=(const u16*)d_in[1]; const u16* wo=(const u16*)d_in[2];
  const u16* wr=(const u16*)d_in[4]; const u16* wg=(const u16*)d_in[5]; const u16* wu=(const u16*)d_in[6]; const u16* wd=(const u16*)d_in[7];
  char* ws=(char*)d_ws; u16* xn=(u16*)ws; u16* qkv=(u16*)(ws+(2<<20)); u16* o=(u16*)(ws+(8<<20)); float* r=(float*)(ws+(10<<20)); float* h=r+4096; float* y=h+4096; int* idx=(int*)(y+4096);
  k_ln<<<512,256,0,stream>>>(x,wr,xn,r);
  k_gemm<0><<<(128*24)/4,256,0,stream>>>(xn,wqkv,1536,qkv,nullptr,nullptr,nullptr,nullptr);
  k_attn<<<32*32,64,0,stream>>>(qkv,o);
  k_moe1<<<6,256,0,stream>>>(r,xn,wg,wu,h,idx);
  k_moe2<<<8,64,0,stream>>>(r,h,wd,idx,y);
  k_gemm<1><<<(128*8)/4,256,0,stream>>>(o,wo,512,(u16*)d_out,x,xn,y,idx);
  for(int i=0;i<EXTRA;i++) k_empty<<<256,256,0,stream>>>(idx);
}
```

```cpp
#include <hip/hip_runtime.h>
#include <hip/hip_bf16.h>
#define STAMP_OFF (16u << 20)
#define STAMP_N 16
static const char* stamp_names[STAMP_N] = {"start", "B0", "B7", "B8", "kv published", "q done", "bar1", "T3 landed", "T3 ok", "attn done", "O published", "wo frags", "O first", "O last", "end", "cycles/100"};
#define FZ_ASM \
  "s_load_dwordx16 s[8:23], %0, 0\ns_load_dwordx2 s[24:25], %0, 64\ns_getpc_b64 s[44:45]\nv_readfirstlane_b32 s27, %2\nv_and_b32 v1, 63, %2\ns_mov_b32 s26, %1\n" \
  "s_lshr_b32 s27, s27, 6\ns_lshr_b32 s42, s26, 5\ns_lshr_b32 s43, s26, 3\nv_lshlrev_b32 v2, 4, v1\ns_cmp_lg_u32 s27, 6\ns_cbranch_scc1 .Lfz_nopf\ns_cmp_ge_u32 s43, 31\n" \
  "s_cbranch_scc1 .Lfz_nopf\ns_lshl_b32 s40, s43, 10\ns_mov_b32 m0, 159744\ns_and_b32 s44, s44, 0xffffff00\nv_add_u32 v6, s40, v2\nglobal_load_lds_dwordx4 v6, s[44:45]\n" \
  ".Lfz_nopf:\ns_and_b32 s40, s26, 4\ns_lshl_b32 s45, s40, 2\ns_lshr_b32 s41, s43, 1\ns_add_u32 s28, s45, s41\ns_and_b32 s44, s26, 3\ns_lshl_b32 s40, s44, 1\n" \
  "s_and_b32 s46, s43, 1\ns_or_b32 s29, s40, s46\ns_lshl_b32 s46, s46, 2\ns_cmp_eq_u32 s29, 3\ns_cselect_b32 s46, 0, s46\ns_add_u32 s46, s46, s41\ns_add_u32 s46, s46, s44\n" \
  "s_and_b32 s30, s46, 7\ns_and_b32 s41, s41, 12\ns_add_u32 s45, s45, s41\ns_add_u32 s1, s45, s44\ns_and_b32 s40, s42, 6\ns_lshl_b32 s44, s44, 1\ns_cmp_lt_u32 s28, 4\n" \
  "s_cselect_b32 s1, s28, s1\ns_cselect_b32 s44, s44, 0\ns_add_u32 s44, s44, s43\ns_and_b32 s2, s44, 7\ns_add_u32 s31, s40, s2\ns_cmp_lg_u32 s27, 7\n" \
  "s_cbranch_scc1 .Lfz_setup\nv_lshrrev_b32 v7, 3, v1\nv_lshrrev_b32 v8, 4, v1\nv_and_b32 v9, 7, v1\nv_xor_b32 v144, v9, v8\nv_lshlrev_b32 v144, 4, v144\n" \
  "v_lshl_or_b32 v144, v7, 10, v144\nv_xor_b32 v145, 64, v144\nv_and_b32 v8, 1, v8\nv_lshlrev_b32 v8, 2, v8\nv_xor_b32 v9, v9, v8\nv_lshlrev_b32 v9, 4, v9\n" \
  "v_mul_u32_u24 v146, 0xc00, v7\nv_add_u32 v146, v146, v9\nv_add_u32 v208, 4096, v144\nv_add_u32 v209, 11264, v145\nv_add_u32 v210, 18432, v144\n" \
  "v_add_u32 v211, 25600, v145\nv_add_u32 v212, 32768, v144\nv_add_u32 v213, 39936, v145\nv_add_u32 v214, 47104, v144\nv_add_u32 v215, 54272, v145\n" \
  "v_add_u32 v216, 4096, v146\nv_add_u32 v217, 27648, v146\nv_add_u32 v218, 51200, v146\nv_add_u32 v219, 74752, v146\nv_add_u32 v220, 98304, v146\n" \
  "v_add_u32 v221, 121856, v146\nv_add_u32 v222, 145408, v146\nv_add_u32 v223, 168960, v146\ns_lshl_b32 s42, s30, 7\nv_add_u32 v147, s42, v2\nv_and_b32 v147, 0x3ff, v147\n" \
  "v_mov_b32 v6, 0\ns_lshl_b32 s40, s28, 16\ns_lshl_b32 s41, s29, 7\ns_mov_b32 m0, 155648\ns_mov_b64 exec, 1\ns_waitcnt lgkmcnt(0)\n" \
  "global_load_lds_dwordx4 v6, s[24:25] sc1\ns_mov_b32 m0, 156672\ns_mov_b64 exec, -1\nglobal_load_lds_dwordx4 v147, s[14:15]\ns_add_u32 s4, s8, s40\ns_addc_u32 s5, s9, 0\n" \
  "s_add_u32 s6, s10, s41\ns_addc_u32 s7, s11, 0\ns_add_u32 s3, s30, 0\ns_and_b32 s3, s3, 7\ns_mul_i32 s3, s3, 196608\ns_mov_b32 m0, 69632\ns_add_u32 s3, s3, 1024\n" \
  "s_add_u32 s46, s6, s3\ns_addc_u32 s47, s7, 0\nglobal_load_lds_dwordx4 v216, s[46:47] offset:-4096\nglobal_load_lds_dwordx4 v217, s[46:47] offset:-3072\n" \
  "global_load_lds_dwordx4 v218, s[46:47] offset:-2048\nglobal_load_lds_dwordx4 v219, s[46:47] offset:-1024\nglobal_load_lds_dwordx4 v220, s[46:47] offset:0\n" \
  "global_load_lds_dwordx4 v221, s[46:47] offset:1024\nglobal_load_lds_dwordx4 v222, s[46:47] offset:2048\nglobal_load_lds_dwordx4 v223, s[46:47] offset:3072\n" \
  "s_add_u32 s3, s30, 0\ns_and_b32 s3, s3, 7\ns_mul_i32 s3, s3, 196608\ns_mov_b32 m0, 77824\ns_add_u32 s3, s3, 2048\ns_add_u32 s46, s6, s3\ns_addc_u32 s47, s7, 0\n" \
  "global_load_lds_dwordx4 v216, s[46:47] offset:-4096\nglobal_load_lds_dwordx4 v217, s[46:47] offset:-3072\nglobal_load_lds_dwordx4 v218, s[46:47] offset:-2048\n" \
  "global_load_lds_dwordx4 v219, s[46:47] offset:-1024\nglobal_load_lds_dwordx4 v220, s[46:47] offset:0\nglobal_load_lds_dwordx4 v221, s[46:47] offset:1024\n" \
  "global_load_lds_dwordx4 v222, s[46:47] offset:2048\nglobal_load_lds_dwordx4 v223, s[46:47] offset:3072\ns_add_u32 s3, s30, 0\ns_and_b32 s3, s3, 7\n" \
  "s_mul_i32 s3, s3, 196608\ns_mov_b32 m0, 143360\ns_add_u32 s46, s6, s3\ns_addc_u32 s47, s7, 0\nglobal_load_lds_dwordx4 v216, s[46:47] offset:-4096\n" \
  "global_load_lds_dwordx4 v217, s[46:47] offset:-3072\nglobal_load_lds_dwordx4 v218, s[46:47] offset:-2048\nglobal_load_lds_dwordx4 v219, s[46:47] offset:-1024\n" \
  "global_load_lds_dwordx4 v220, s[46:47] offset:0\nglobal_load_lds_dwordx4 v221, s[46:47] offset:1024\nglobal_load_lds_dwordx4 v222, s[46:47] offset:2048\n" \
  "global_load_lds_dwordx4 v223, s[46:47] offset:3072\ns_add_u32 s3, s30, 0\ns_and_b32 s3, s3, 7\ns_lshl_b32 s3, s3, 7\ns_mov_b32 m0, 4096\ns_add_u32 s46, s4, s3\n" \
  "s_addc_u32 s47, s5, 0\nglobal_load_lds_dwordx4 v208, s[46:47] offset:-4096\nglobal_load_lds_dwordx4 v209, s[46:47] offset:-3072\n" \
  "global_load_lds_dwordx4 v210, s[46:47] offset:-2048\nglobal_load_lds_dwordx4 v211, s[46:47] offset:-1024\nglobal_load_lds_dwordx4 v212, s[46:47] offset:0\n" \
  "global_load_lds_dwordx4 v213, s[46:47] offset:1024\nglobal_load_lds_dwordx4 v214, s[46:47] offset:2048\nglobal_load_lds_dwordx4 v215, s[46:47] offset:3072\n" \
  "s_add_u32 s3, s30, 1\ns_and_b32 s3, s3, 7\ns_lshl_b32 s3, s3, 7\ns_mov_b32 m0, 12288\ns_add_u32 s46, s4, s3\ns_addc_u32 s47, s5, 0\n" \
  "global_load_lds_dwordx4 v208, s[46:47] offset:-4096\nglobal_load_lds_dwordx4 v209, s[46:47] offset:-3072\nglobal_load_lds_dwordx4 v210, s[46:47] offset:-2048\n" \
  "global_load_lds_dwordx4 v211, s[46:47] offset:-1024\nglobal_load_lds_dwordx4 v212, s[46:47] offset:0\nglobal_load_lds_dwordx4 v213, s[46:47] offset:1024\n" \
  "global_load_lds_dwordx4 v214, s[46:47] offset:2048\nglobal_load_lds_dwordx4 v215, s[46:47] offset:3072\ns_add_u32 s3, s30, 1\ns_and_b32 s3, s3, 7\n" \
  "s_mul_i32 s3, s3, 196608\ns_mov_b32 m0, 86016\ns_add_u32 s3, s3, 1024\ns_add_u32 s46, s6, s3\ns_addc_u32 s47, s7, 0\nglobal_load_lds_dwordx4 v216, s[46:47] offset:-4096\n" \
  "global_load_lds_dwordx4 v217, s[46:47] offset:-3072\nglobal_load_lds_dwordx4 v218, s[46:47] offset:-2048\nglobal_load_lds_dwordx4 v219, s[46:47] offset:-1024\n" \
  "s_and_b32 s33, s27, 3\ns_xor_b32 s0, s27, 4\ns_lshr_b32 s34, s0, 2\ns_sub_u32 s32, s28, 4\nv_lshrrev_b32 v7, 5, v1\nv_and_b32 v6, 31, v1\nv_lshlrev_b32 v7, 2, v7\n" \
  "v_sub_u32 v6, v6, v7\nv_mov_b32 v7, 0xff800000\nv_cmp_le_i32 s[40:41], 0, v6\nv_cmp_le_i32 s[42:43], 1, v6\nv_cmp_le_i32 s[44:45], 2, v6\n" \
  "v_cndmask_b32 v16, v7, 0, s[40:41]\nv_cndmask_b32 v17, v7, 0, s[42:43]\nv_cndmask_b32 v18, v7, 0, s[44:45]\nv_cmp_le_i32 s[40:41], 3, v6\nv_cmp_le_i32 s[42:43], 8, v6\n" \
  "v_cmp_le_i32 s[44:45], 9, v6\nv_cndmask_b32 v19, v7, 0, s[40:41]\nv_cndmask_b32 v20, v7, 0, s[42:43]\nv_cndmask_b32 v21, v7, 0, s[44:45]\nv_cmp_le_i32 s[40:41], 10, v6\n" \
  "v_cmp_le_i32 s[42:43], 11, v6\nv_cmp_le_i32 s[44:45], 16, v6\nv_cndmask_b32 v22, v7, 0, s[40:41]\nv_cndmask_b32 v23, v7, 0, s[42:43]\nv_cndmask_b32 v24, v7, 0, s[44:45]\n" \
  "v_cmp_le_i32 s[40:41], 17, v6\nv_cmp_le_i32 s[42:43], 18, v6\nv_cmp_le_i32 s[44:45], 19, v6\nv_cndmask_b32 v25, v7, 0, s[40:41]\nv_cndmask_b32 v26, v7, 0, s[42:43]\n" \
  "v_cndmask_b32 v27, v7, 0, s[44:45]\nv_cmp_le_i32 s[40:41], 24, v6\nv_cmp_le_i32 s[42:43], 25, v6\nv_cmp_le_i32 s[44:45], 26, v6\nv_cndmask_b32 v28, v7, 0, s[40:41]\n" \
  "v_cndmask_b32 v29, v7, 0, s[42:43]\nv_cndmask_b32 v30, v7, 0, s[44:45]\nv_cmp_le_i32 s[40:41], 27, v6\ns_nop 1\nv_cndmask_b32 v31, v7, 0, s[40:41]\n" \
  "v_mov_b32 v48, 0x3f803f80\nv_mov_b32 v49, v48\nv_mov_b64 v[50:51], v[48:49]\nv_mov_b64 v[52:53], v[48:49]\nv_mov_b64 v[54:55], v[48:49]\nv_mov_b64 v[56:57], v[48:49]\n" \
  "v_mov_b64 v[58:59], v[48:49]\nv_mov_b64 v[60:61], v[48:49]\nv_mov_b64 v[62:63], v[48:49]\nv_mov_b64 v[64:65], v[48:49]\nv_mov_b64 v[66:67], v[48:49]\n" \
  "v_mov_b64 v[68:69], 0\nv_mov_b64 v[70:71], 0\nv_mov_b64 v[72:73], 0\nv_mov_b64 v[74:75], 0\nv_lshrrev_b32 v128, 4, v1\nv_and_b32 v129, 1, v128\n" \
  "v_lshrrev_b32 v130, 1, v128\nv_and_b32 v131, 15, v1\nv_bfe_u32 v132, v1, 2, 2\ns_lshl_b32 s40, s33, 4\nv_add_u32 v133, s40, v131\nv_lshrrev_b32 v6, 3, v1\n" \
  "v_and_b32 v9, 7, v1\nv_lshlrev_b32 v8, 1, v128\nv_xor_b32 v9, v9, v8\nv_lshlrev_b32 v9, 4, v9\nv_lshl_or_b32 v10, v6, 10, v9\ns_lshl_b32 s40, s34, 12\n" \
  "s_lshl_b32 s41, s33, 9\ns_add_u32 s40, s40, s41\ns_add_u32 s40, s40, 16384\nv_lshlrev_b32 v6, 4, v131\nv_lshl_or_b32 v6, v130, 8, v6\nv_lshl_or_b32 v6, v129, 11, v6\n" \
  "v_add_u32 v11, s40, v6\ns_lshl_b32 s40, s0, 6\ns_add_u32 s40, s40, 25600\nv_lshl_add_u32 v84, v128, 4, s40\nv_and_b32 v7, 1, v1\nv_lshlrev_b32 v6, 2, v132\n" \
  "v_lshl_or_b32 v6, v128, 5, v6\nv_lshl_or_b32 v6, v7, 7, v6\ns_lshr_b32 s40, s33, 1\ns_lshl_b32 s40, s40, 8\ns_and_b32 s41, s33, 1\ns_lshl_b32 s41, s41, 4\n" \
  "s_add_u32 s40, s40, s41\ns_lshl_b32 s41, s33, 11\ns_sub_u32 s40, s40, s41\ns_add_u32 s40, s40, 9216\nv_add_u32 v85, s40, v6\nv_lshlrev_b32 v6, 2, v128\n" \
  "v_lshl_or_b32 v6, v132, 4, v6\nv_lshl_or_b32 v6, v7, 6, v6\ns_mul_i32 s40, s0, 1920\ns_sub_u32 s40, 24576, s40\nv_add_u32 v3, s40, v6\n" \
  "global_load_lds_dwordx4 v220, s[46:47] offset:0\nglobal_load_lds_dwordx4 v221, s[46:47] offset:1024\nglobal_load_lds_dwordx4 v222, s[46:47] offset:2048\n" \
  "global_load_lds_dwordx4 v223, s[46:47] offset:3072\ns_add_u32 s3, s30, 1\ns_and_b32 s3, s3, 7\ns_mul_i32 s3, s3, 196608\ns_mov_b32 m0, 94208\ns_add_u32 s3, s3, 2048\n" \
  "s_add_u32 s46, s6, s3\ns_addc_u32 s47, s7, 0\nglobal_load_lds_dwordx4 v216, s[46:47] offset:-4096\nglobal_load_lds_dwordx4 v217, s[46:47] offset:-3072\n" \
  "global_load_lds_dwordx4 v218, s[46:47] offset:-2048\nglobal_load_lds_dwordx4 v219, s[46:47] offset:-1024\nglobal_load_lds_dwordx4 v220, s[46:47] offset:0\n" \
  "global_load_lds_dwordx4 v221, s[46:47] offset:1024\nglobal_load_lds_dwordx4 v222, s[46:47] offset:2048\nglobal_load_lds_dwordx4 v223, s[46:47] offset:3072\n" \
  "s_add_u32 s3, s30, 2\ns_and_b32 s3, s3, 7\ns_lshl_b32 s3, s3, 7\ns_mov_b32 m0, 20480\ns_add_u32 s46, s4, s3\ns_addc_u32 s47, s5, 0\n" \
  "global_load_lds_dwordx4 v208, s[46:47] offset:-4096\nglobal_load_lds_dwordx4 v209, s[46:47] offset:-3072\nglobal_load_lds_dwordx4 v210, s[46:47] offset:-2048\n" \
  "global_load_lds_dwordx4 v211, s[46:47] offset:-1024\nglobal_load_lds_dwordx4 v212, s[46:47] offset:0\nglobal_load_lds_dwordx4 v213, s[46:47] offset:1024\n" \
  "global_load_lds_dwordx4 v214, s[46:47] offset:2048\nglobal_load_lds_dwordx4 v215, s[46:47] offset:3072\ns_add_u32 s3, s30, 2\ns_and_b32 s3, s3, 7\n" \
  "s_mul_i32 s3, s3, 196608\ns_mov_b32 m0, 102400\ns_add_u32 s3, s3, 1024\ns_add_u32 s46, s6, s3\ns_addc_u32 s47, s7, 0\n" \
  "global_load_lds_dwordx4 v216, s[46:47] offset:-4096\nglobal_load_lds_dwordx4 v217, s[46:47] offset:-3072\nglobal_load_lds_dwordx4 v218, s[46:47] offset:-2048\n" \
  "global_load_lds_dwordx4 v219, s[46:47] offset:-1024\nglobal_load_lds_dwordx4 v220, s[46:47] offset:0\nglobal_load_lds_dwordx4 v221, s[46:47] offset:1024\n" \
  "global_load_lds_dwordx4 v222, s[46:47] offset:2048\nglobal_load_lds_dwordx4 v223, s[46:47] offset:3072\ns_waitcnt vmcnt(40)\ns_barrier\nv_mov_b32 v4, 155648\n" \
  "ds_read_b64 v[4:5], v4\ns_add_u32 s3, s30, 2\ns_and_b32 s3, s3, 7\ns_mul_i32 s3, s3, 196608\ns_mov_b32 m0, 110592\ns_add_u32 s3, s3, 2048\ns_add_u32 s46, s6, s3\n" \
  "s_addc_u32 s47, s7, 0\nglobal_load_lds_dwordx4 v216, s[46:47] offset:-4096\nglobal_load_lds_dwordx4 v217, s[46:47] offset:-3072\n" \
  "global_load_lds_dwordx4 v218, s[46:47] offset:-2048\nglobal_load_lds_dwordx4 v219, s[46:47] offset:-1024\nglobal_load_lds_dwordx4 v220, s[46:47] offset:0\n" \
  "global_load_lds_dwordx4 v221, s[46:47] offset:1024\nglobal_load_lds_dwordx4 v222, s[46:47] offset:2048\nglobal_load_lds_dwordx4 v223, s[46:47] offset:3072\n" \
  "s_add_u32 s3, s30, 3\ns_and_b32 s3, s3, 7\ns_lshl_b32 s3, s3, 7\ns_mov_b32 m0, 28672\ns_add_u32 s46, s4, s3\ns_addc_u32 s47, s5, 0\n" \
  "global_load_lds_dwordx4 v208, s[46:47] offset:-4096\nglobal_load_lds_dwordx4 v209, s[46:47] offset:-3072\nglobal_load_lds_dwordx4 v210, s[46:47] offset:-2048\n" \
  "global_load_lds_dwordx4 v211, s[46:47] offset:-1024\nglobal_load_lds_dwordx4 v212, s[46:47] offset:0\nglobal_load_lds_dwordx4 v213, s[46:47] offset:1024\n" \
  "global_load_lds_dwordx4 v214, s[46:47] offset:2048\nglobal_load_lds_dwordx4 v215, s[46:47] offset:3072\ns_add_u32 s3, s30, 4\ns_and_b32 s3, s3, 7\ns_lshl_b32 s3, s3, 7\n" \
  "s_mov_b32 m0, 36864\ns_add_u32 s46, s4, s3\ns_addc_u32 s47, s5, 0\nglobal_load_lds_dwordx4 v208, s[46:47] offset:-4096\n" \
  "global_load_lds_dwordx4 v209, s[46:47] offset:-3072\nglobal_load_lds_dwordx4 v210, s[46:47] offset:-2048\nglobal_load_lds_dwordx4 v211, s[46:47] offset:-1024\n" \
  "global_load_lds_dwordx4 v212, s[46:47] offset:0\nglobal_load_lds_dwordx4 v213, s[46:47] offset:1024\nglobal_load_lds_dwordx4 v214, s[46:47] offset:2048\n" \
  "global_load_lds_dwordx4 v215, s[46:47] offset:3072\ns_mul_i32 s40, s33, 1792\ns_sub_u32 s40, 9728, s40\nv_lshl_add_u32 v15, v131, 4, s40\ns_lshl_b32 s41, s34, 6\n" \
  "v_lshlrev_b32 v6, 10, v133\nv_lshl_or_b32 v6, v129, 5, v6\nv_lshl_or_b32 v6, v130, 4, v6\nv_or_b32 v12, s41, v6\ns_lshl_b32 s41, s0, 11\ns_add_u32 s41, s41, 81920\n" \
  "v_add_u32 v14, s41, v2\nv_xor_b32 v13, 0x2000, v14\ns_lshl_b32 s40, s27, 8\ns_add_u32 s40, s40, 157696\nv_lshl_add_u32 v86, v1, 2, s40\nv_lshlrev_b32 v7, 4, v130\n" \
  "v_lshl_or_b32 v7, v129, 2, v7\nv_or_b32 v7, v7, v132\nv_xor_b32 v9, s34, v129\nv_lshrrev_b32 v8, 1, v132\nv_lshl_or_b32 v9, v9, 1, v8\nv_and_b32 v8, 3, v1\n" \
  "v_lshlrev_b32 v8, 3, v8\nv_lshl_or_b32 v7, v7, 7, v8\ns_lshl_b32 s40, s34, 15\nv_lshl_or_b32 v7, v9, 5, v7\nv_or_b32 v76, s40, v7\nv_lshlrev_b32 v7, 2, v129\n" \
  "v_cmp_eq_u32 vcc, v7, v131\nv_xor_b32 v77, 32, v76\nv_xor_b32 v78, 64, v76\nv_xor_b32 v79, 96, v76\nv_cndmask_b32 v228, 0, v48, vcc\nv_cndmask_b32 v229, 0, v48, vcc\n" \
  "v_cndmask_b32 v230, 0, v48, vcc\nv_cndmask_b32 v231, 0, v48, vcc\nv_and_b32 v6, 31, v1\nv_bfe_u32 v8, v1, 1, 3\nv_xor_b32 v8, v8, v130\nv_lshlrev_b32 v8, 4, v8\n" \
  "v_lshl_or_b32 v8, v6, 7, v8\ns_cmp_ge_u32 s27, 6\ns_cselect_b32 s40, 4096, 0\ns_cselect_b32 s42, 4160, 0\nv_xor_b32 v248, s42, v8\ns_mov_b32 s42, s40\n" \
  "s_lshr_b32 s40, s40, 5\ns_add_u32 s40, s40, 155904\nv_lshl_add_u32 v254, v6, 2, s40\nv_xor_b32 v249, 32, v248\ns_waitcnt vmcnt(32)\ns_barrier\ns_waitcnt lgkmcnt(0)\n" \
  "v_readfirstlane_b32 s48, v4\nv_readfirstlane_b32 s49, v5\ns_add_u32 s48, s48, 1\ns_addc_u32 s49, s49, 0\ns_mul_i32 s35, s48, 0x9e3779b1\ns_mul_i32 s36, s49, 0x85ebca77\n" \
  "s_add_u32 s35, s35, s36\ns_lshl_b32 s36, s35, 1\ns_add_u32 s3, s30, 5\ns_and_b32 s3, s3, 7\ns_lshl_b32 s3, s3, 7\ns_mov_b32 m0, 45056\ns_add_u32 s46, s4, s3\n" \
  "s_addc_u32 s47, s5, 0\nglobal_load_lds_dwordx4 v208, s[46:47] offset:-4096\nglobal_load_lds_dwordx4 v209, s[46:47] offset:-3072\n" \
  "global_load_lds_dwordx4 v210, s[46:47] offset:-2048\nglobal_load_lds_dwordx4 v211, s[46:47] offset:-1024\nglobal_load_lds_dwordx4 v212, s[46:47] offset:0\n" \
  "global_load_lds_dwordx4 v213, s[46:47] offset:1024\nglobal_load_lds_dwordx4 v214, s[46:47] offset:2048\nglobal_load_lds_dwordx4 v215, s[46:47] offset:3072\n" \
  "s_add_u32 s3, s30, 3\ns_and_b32 s3, s3, 7\ns_mul_i32 s3, s3, 196608\ns_mov_b32 m0, 118784\ns_add_u32 s3, s3, 1024\ns_add_u32 s46, s6, s3\ns_addc_u32 s47, s7, 0\n" \
  "global_load_lds_dwordx4 v216, s[46:47] offset:-4096\nglobal_load_lds_dwordx4 v217, s[46:47] offset:-3072\nglobal_load_lds_dwordx4 v218, s[46:47] offset:-2048\n" \
  "global_load_lds_dwordx4 v219, s[46:47] offset:-1024\nglobal_load_lds_dwordx4 v220, s[46:47] offset:0\nglobal_load_lds_dwordx4 v221, s[46:47] offset:1024\n" \
  "global_load_lds_dwordx4 v222, s[46:47] offset:2048\nglobal_load_lds_dwordx4 v223, s[46:47] offset:3072\ns_add_u32 s3, s30, 3\ns_and_b32 s3, s3, 7\n" \
  "s_mul_i32 s3, s3, 196608\ns_mov_b32 m0, 126976\ns_add_u32 s3, s3, 2048\ns_add_u32 s46, s6, s3\ns_addc_u32 s47, s7, 0\n" \
  "global_load_lds_dwordx4 v216, s[46:47] offset:-4096\nglobal_load_lds_dwordx4 v217, s[46:47] offset:-3072\nglobal_load_lds_dwordx4 v218, s[46:47] offset:-2048\n" \
  "global_load_lds_dwordx4 v219, s[46:47] offset:-1024\nglobal_load_lds_dwordx4 v220, s[46:47] offset:0\nglobal_load_lds_dwordx4 v221, s[46:47] offset:1024\n" \
  "global_load_lds_dwordx4 v222, s[46:47] offset:2048\nglobal_load_lds_dwordx4 v223, s[46:47] offset:3072\ns_waitcnt vmcnt(24)\ns_barrier\ns_add_u32 s3, s30, 6\n" \
  "s_and_b32 s3, s3, 7\ns_lshl_b32 s3, s3, 7\ns_mov_b32 m0, 53248\ns_add_u32 s46, s4, s3\ns_addc_u32 s47, s5, 0\nglobal_load_lds_dwordx4 v208, s[46:47] offset:-4096\n" \
  "global_load_lds_dwordx4 v209, s[46:47] offset:-3072\nglobal_load_lds_dwordx4 v210, s[46:47] offset:-2048\nglobal_load_lds_dwordx4 v211, s[46:47] offset:-1024\n" \
  "global_load_lds_dwordx4 v212, s[46:47] offset:0\nglobal_load_lds_dwordx4 v213, s[46:47] offset:1024\nglobal_load_lds_dwordx4 v214, s[46:47] offset:2048\n" \
  "global_load_lds_dwordx4 v215, s[46:47] offset:3072\ns_add_u32 s3, s30, 4\ns_and_b32 s3, s3, 7\ns_mul_i32 s3, s3, 196608\ns_mov_b32 m0, 69632\ns_add_u32 s3, s3, 1024\n" \
  "s_add_u32 s46, s6, s3\ns_addc_u32 s47, s7, 0\nglobal_load_lds_dwordx4 v216, s[46:47] offset:-4096\nglobal_load_lds_dwordx4 v217, s[46:47] offset:-3072\n" \
  "global_load_lds_dwordx4 v218, s[46:47] offset:-2048\nglobal_load_lds_dwordx4 v219, s[46:47] offset:-1024\nglobal_load_lds_dwordx4 v220, s[46:47] offset:0\n" \
  "global_load_lds_dwordx4 v221, s[46:47] offset:1024\nglobal_load_lds_dwordx4 v222, s[46:47] offset:2048\nglobal_load_lds_dwordx4 v223, s[46:47] offset:3072\n" \
  "s_add_u32 s3, s30, 4\ns_and_b32 s3, s3, 7\ns_mul_i32 s3, s3, 196608\ns_mov_b32 m0, 77824\ns_add_u32 s3, s3, 2048\ns_add_u32 s46, s6, s3\ns_addc_u32 s47, s7, 0\n" \
  "global_load_lds_dwordx4 v216, s[46:47] offset:-4096\nglobal_load_lds_dwordx4 v217, s[46:47] offset:-3072\nglobal_load_lds_dwordx4 v218, s[46:47] offset:-2048\n" \
  "global_load_lds_dwordx4 v219, s[46:47] offset:-1024\nglobal_load_lds_dwordx4 v220, s[46:47] offset:0\nglobal_load_lds_dwordx4 v221, s[46:47] offset:1024\n" \
  "global_load_lds_dwordx4 v222, s[46:47] offset:2048\nglobal_load_lds_dwordx4 v223, s[46:47] offset:3072\nv_xor_b32 v250, 64, v248\nv_xor_b32 v251, 0x60, v248\n" \
  "v_lshl_or_b32 v6, v130, 3, v132\nv_lshlrev_b32 v6, 7, v6\nv_bfe_u32 v8, v1, 1, 1\nv_lshl_or_b32 v8, v129, 1, v8\nv_bfe_u32 v9, v1, 3, 1\ns_and_b32 s40, s27, 1\n" \
  "v_xor_b32 v9, s40, v9\nv_lshl_or_b32 v8, v9, 2, v8\nv_lshl_or_b32 v6, v8, 4, v6\nv_and_b32 v8, 1, v1\nv_lshl_or_b32 v6, v8, 3, v6\nv_or_b32 v6, s42, v6\n" \
  "v_add_u32 v252, 65536, v6\nv_add_u32 v253, 114688, v6\ns_mov_b32 s50, 0x33333333\ns_mov_b32 s51, 0x33333333\ns_lshl_b32 s40, s28, 3\ns_mul_i32 s40, s40, 27136\n" \
  "s_add_u32 s40, s40, 1048576\ns_mul_i32 s41, s29, 27136\ns_add_u32 s40, s40, s41\ns_add_u32 s38, s24, s40\ns_addc_u32 s39, s25, 0\ns_mul_i32 s41, s1, 217088\n" \
  "s_lshl_b32 s40, s33, 11\ns_add_u32 s40, s40, s41\ns_add_u32 s40, s40, 1064960\ns_add_u32 s42, s24, s40\ns_addc_u32 s43, s25, 0\ns_waitcnt vmcnt(24)\ns_barrier\n" \
  "s_add_u32 s3, s30, 7\ns_and_b32 s3, s3, 7\ns_lshl_b32 s3, s3, 7\ns_mov_b32 m0, 61440\ns_add_u32 s46, s4, s3\ns_addc_u32 s47, s5, 0\n" \
  "global_load_lds_dwordx4 v208, s[46:47] offset:-4096\nglobal_load_lds_dwordx4 v209, s[46:47] offset:-3072\nglobal_load_lds_dwordx4 v210, s[46:47] offset:-2048\n" \
  "global_load_lds_dwordx4 v211, s[46:47] offset:-1024\nglobal_load_lds_dwordx4 v212, s[46:47] offset:0\nglobal_load_lds_dwordx4 v213, s[46:47] offset:1024\n" \
  "global_load_lds_dwordx4 v214, s[46:47] offset:2048\nglobal_load_lds_dwordx4 v215, s[46:47] offset:3072\ns_add_u32 s3, s30, 5\ns_and_b32 s3, s3, 7\n" \
  "s_mul_i32 s3, s3, 196608\ns_mov_b32 m0, 86016\ns_add_u32 s3, s3, 1024\ns_add_u32 s46, s6, s3\ns_addc_u32 s47, s7, 0\nglobal_load_lds_dwordx4 v216, s[46:47] offset:-4096\n" \
  "global_load_lds_dwordx4 v217, s[46:47] offset:-3072\nglobal_load_lds_dwordx4 v218, s[46:47] offset:-2048\nglobal_load_lds_dwordx4 v219, s[46:47] offset:-1024\n" \
  "global_load_lds_dwordx4 v220, s[46:47] offset:0\nglobal_load_lds_dwordx4 v221, s[46:47] offset:1024\nglobal_load_lds_dwordx4 v222, s[46:47] offset:2048\n" \
  "global_load_lds_dwordx4 v223, s[46:47] offset:3072\ns_add_u32 s3, s30, 5\ns_and_b32 s3, s3, 7\ns_mul_i32 s3, s3, 196608\ns_mov_b32 m0, 94208\ns_add_u32 s3, s3, 2048\n" \
  "s_add_u32 s46, s6, s3\ns_addc_u32 s47, s7, 0\nglobal_load_lds_dwordx4 v216, s[46:47] offset:-4096\nglobal_load_lds_dwordx4 v217, s[46:47] offset:-3072\n" \
  "global_load_lds_dwordx4 v218, s[46:47] offset:-2048\nglobal_load_lds_dwordx4 v219, s[46:47] offset:-1024\nglobal_load_lds_dwordx4 v220, s[46:47] offset:0\n" \
  "global_load_lds_dwordx4 v221, s[46:47] offset:1024\nglobal_load_lds_dwordx4 v222, s[46:47] offset:2048\nglobal_load_lds_dwordx4 v223, s[46:47] offset:3072\n" \
  "s_lshl_b32 s44, s34, 2\ns_add_u32 s44, s44, s31\ns_add_u32 s40, s44, 0\ns_and_b32 s40, s40, 7\ns_mul_i32 s40, s40, 27136\ns_add_u32 s60, s42, s40\n" \
  "s_addc_u32 s61, s43, 0\ns_add_u32 s40, s44, 1\ns_and_b32 s40, s40, 7\ns_mul_i32 s40, s40, 27136\ns_add_u32 s62, s42, s40\ns_addc_u32 s63, s43, 0\ns_add_u32 s40, s44, 2\n" \
  "s_and_b32 s40, s40, 7\ns_mul_i32 s40, s40, 27136\ns_add_u32 s64, s42, s40\ns_addc_u32 s65, s43, 0\ns_add_u32 s40, s44, 3\ns_and_b32 s40, s40, 7\n" \
  "s_mul_i32 s40, s40, 27136\ns_add_u32 s66, s42, s40\ns_addc_u32 s67, s43, 0\ns_lshl_b32 s40, s0, 11\ns_add_u32 s42, s38, s40\ns_addc_u32 s43, s39, 0\n" \
  "s_min_u32 s40, s28, 4\ns_mul_i32 s40, s40, 217088\ns_sub_u32 s52, s42, s40\ns_subb_u32 s53, s43, 0\ns_min_u32 s40, s28, 3\ns_waitcnt vmcnt(24)\ns_barrier\n" \
  "s_add_u32 s3, s30, 6\ns_and_b32 s3, s3, 7\ns_mul_i32 s3, s3, 196608\ns_mov_b32 m0, 102400\ns_add_u32 s3, s3, 1024\ns_add_u32 s46, s6, s3\ns_addc_u32 s47, s7, 0\n" \
  "global_load_lds_dwordx4 v216, s[46:47] offset:-4096\nglobal_load_lds_dwordx4 v217, s[46:47] offset:-3072\nglobal_load_lds_dwordx4 v218, s[46:47] offset:-2048\n" \
  "global_load_lds_dwordx4 v219, s[46:47] offset:-1024\nglobal_load_lds_dwordx4 v220, s[46:47] offset:0\nglobal_load_lds_dwordx4 v221, s[46:47] offset:1024\n" \
  "global_load_lds_dwordx4 v222, s[46:47] offset:2048\nglobal_load_lds_dwordx4 v223, s[46:47] offset:3072\ns_add_u32 s3, s30, 6\ns_and_b32 s3, s3, 7\n" \
  "s_mul_i32 s3, s3, 196608\ns_mov_b32 m0, 110592\ns_add_u32 s3, s3, 2048\ns_add_u32 s46, s6, s3\ns_addc_u32 s47, s7, 0\n" \
  "global_load_lds_dwordx4 v216, s[46:47] offset:-4096\nglobal_load_lds_dwordx4 v217, s[46:47] offset:-3072\nglobal_load_lds_dwordx4 v218, s[46:47] offset:-2048\n" \
  "global_load_lds_dwordx4 v219, s[46:47] offset:-1024\nglobal_load_lds_dwordx4 v220, s[46:47] offset:0\nglobal_load_lds_dwordx4 v221, s[46:47] offset:1024\n" \
  "global_load_lds_dwordx4 v222, s[46:47] offset:2048\nglobal_load_lds_dwordx4 v223, s[46:47] offset:3072\ns_mul_i32 s40, s40, 217088\ns_sub_u32 s54, s42, s40\n" \
  "s_subb_u32 s55, s43, 0\ns_min_u32 s40, s28, 2\ns_mul_i32 s40, s40, 217088\ns_sub_u32 s56, s42, s40\ns_subb_u32 s57, s43, 0\ns_min_u32 s40, s28, 1\n" \
  "s_mul_i32 s40, s40, 217088\ns_sub_u32 s58, s42, s40\ns_subb_u32 s59, s43, 0\ns_lshl_b32 s40, s2, 7\ns_lshl_b32 s41, s27, 13\ns_add_u32 s41, s41, s40\n" \
  "s_add_u32 s12, s12, s41\ns_addc_u32 s13, s13, 0\ns_lshl_b32 s41, s1, 16\ns_add_u32 s41, s41, s40\ns_add_u32 s22, s22, s41\ns_addc_u32 s23, s23, 0\n" \
  "s_lshl_b32 s40, s28, 3\ns_add_u32 s40, s40, s29\ns_lshl_b32 s41, s40, 1\ns_add_u32 s42, s40, 19\ns_cmp_lt_u32 s40, 24\ns_cselect_b32 s11, 1, 0\ns_cmp_lt_u32 s40, 19\n" \
  "s_cselect_b32 s11, 2, s11\ns_cselect_b32 s14, s41, s42\ns_lshl_b32 s41, s14, 15\ns_waitcnt vmcnt(16)\ns_barrier\ns_add_u32 s3, s30, 7\ns_and_b32 s3, s3, 7\n" \
  "s_mul_i32 s3, s3, 196608\ns_mov_b32 m0, 118784\ns_add_u32 s3, s3, 1024\ns_add_u32 s46, s6, s3\ns_addc_u32 s47, s7, 0\n" \
  "global_load_lds_dwordx4 v216, s[46:47] offset:-4096\nglobal_load_lds_dwordx4 v217, s[46:47] offset:-3072\nglobal_load_lds_dwordx4 v218, s[46:47] offset:-2048\n" \
  "global_load_lds_dwordx4 v219, s[46:47] offset:-1024\nglobal_load_lds_dwordx4 v220, s[46:47] offset:0\nglobal_load_lds_dwordx4 v221, s[46:47] offset:1024\n" \
  "global_load_lds_dwordx4 v222, s[46:47] offset:2048\nglobal_load_lds_dwordx4 v223, s[46:47] offset:3072\ns_add_u32 s3, s30, 7\ns_and_b32 s3, s3, 7\n" \
  "s_mul_i32 s3, s3, 196608\ns_mov_b32 m0, 126976\ns_add_u32 s3, s3, 2048\ns_add_u32 s46, s6, s3\ns_addc_u32 s47, s7, 0\n" \
  "global_load_lds_dwordx4 v216, s[46:47] offset:-4096\nglobal_load_lds_dwordx4 v217, s[46:47] offset:-3072\nglobal_load_lds_dwordx4 v218, s[46:47] offset:-2048\n" \
  "global_load_lds_dwordx4 v219, s[46:47] offset:-1024\nglobal_load_lds_dwordx4 v220, s[46:47] offset:0\nglobal_load_lds_dwordx4 v221, s[46:47] offset:1024\n" \
  "global_load_lds_dwordx4 v222, s[46:47] offset:2048\nglobal_load_lds_dwordx4 v223, s[46:47] offset:3072\ns_add_u32 s3, s30, 1\ns_and_b32 s3, s3, 7\n" \
  "s_mul_i32 s3, s3, 196608\ns_mov_b32 m0, 151552\ns_add_u32 s46, s6, s3\ns_addc_u32 s47, s7, 0\nglobal_load_lds_dwordx4 v216, s[46:47] offset:-4096\n" \
  "global_load_lds_dwordx4 v217, s[46:47] offset:-3072\nglobal_load_lds_dwordx4 v218, s[46:47] offset:-2048\nglobal_load_lds_dwordx4 v219, s[46:47] offset:-1024\n" \
  "s_add_u32 s20, s20, s41\ns_addc_u32 s21, s21, 0\nv_lshrrev_b32 v6, 2, v1\nv_xor_b32 v6, s33, v6\nv_and_b32 v7, 3, v1\nv_lshlrev_b32 v7, 4, v7\n" \
  "v_lshl_or_b32 v87, v6, 6, v7\ns_waitcnt vmcnt(20)\ns_barrier\nglobal_load_lds_dwordx4 v220, s[46:47] offset:0\nglobal_load_lds_dwordx4 v221, s[46:47] offset:1024\n" \
  "global_load_lds_dwordx4 v222, s[46:47] offset:2048\nglobal_load_lds_dwordx4 v223, s[46:47] offset:3072\ns_add_u32 s3, s30, 2\ns_and_b32 s3, s3, 7\n" \
  "s_mul_i32 s3, s3, 196608\ns_mov_b32 m0, 135168\ns_add_u32 s46, s6, s3\ns_addc_u32 s47, s7, 0\nglobal_load_lds_dwordx4 v216, s[46:47] offset:-4096\n" \
  "global_load_lds_dwordx4 v217, s[46:47] offset:-3072\nglobal_load_lds_dwordx4 v218, s[46:47] offset:-2048\nglobal_load_lds_dwordx4 v219, s[46:47] offset:-1024\n" \
  "global_load_lds_dwordx4 v220, s[46:47] offset:0\nglobal_load_lds_dwordx4 v221, s[46:47] offset:1024\nglobal_load_lds_dwordx4 v222, s[46:47] offset:2048\n" \
  "global_load_lds_dwordx4 v223, s[46:47] offset:3072\ns_add_u32 s3, s30, 3\ns_and_b32 s3, s3, 7\ns_mul_i32 s3, s3, 196608\ns_mov_b32 m0, 69632\ns_add_u32 s46, s6, s3\n" \
  "s_addc_u32 s47, s7, 0\nglobal_load_lds_dwordx4 v216, s[46:47] offset:-4096\nglobal_load_lds_dwordx4 v217, s[46:47] offset:-3072\ns_waitcnt vmcnt(18)\ns_barrier\n" \
  "global_load_lds_dwordx4 v218, s[46:47] offset:-2048\nglobal_load_lds_dwordx4 v219, s[46:47] offset:-1024\nglobal_load_lds_dwordx4 v220, s[46:47] offset:0\n" \
  "global_load_lds_dwordx4 v221, s[46:47] offset:1024\nglobal_load_lds_dwordx4 v222, s[46:47] offset:2048\nglobal_load_lds_dwordx4 v223, s[46:47] offset:3072\n" \
  "s_add_u32 s3, s30, 4\ns_and_b32 s3, s3, 7\ns_mul_i32 s3, s3, 196608\ns_mov_b32 m0, 77824\ns_add_u32 s46, s6, s3\ns_addc_u32 s47, s7, 0\n" \
  "global_load_lds_dwordx4 v216, s[46:47] offset:-4096\nglobal_load_lds_dwordx4 v217, s[46:47] offset:-3072\nglobal_load_lds_dwordx4 v218, s[46:47] offset:-2048\n" \
  "global_load_lds_dwordx4 v219, s[46:47] offset:-1024\nglobal_load_lds_dwordx4 v220, s[46:47] offset:0\nglobal_load_lds_dwordx4 v221, s[46:47] offset:1024\n" \
  "global_load_lds_dwordx4 v222, s[46:47] offset:2048\nglobal_load_lds_dwordx4 v223, s[46:47] offset:3072\ns_add_u32 s3, s30, 5\ns_and_b32 s3, s3, 7\n" \
  "s_mul_i32 s3, s3, 196608\ns_mov_b32 m0, 86016\ns_add_u32 s46, s6, s3\ns_addc_u32 s47, s7, 0\nglobal_load_lds_dwordx4 v216, s[46:47] offset:-4096\n" \
  "global_load_lds_dwordx4 v217, s[46:47] offset:-3072\nglobal_load_lds_dwordx4 v218, s[46:47] offset:-2048\nglobal_load_lds_dwordx4 v219, s[46:47] offset:-1024\n" \
  "global_load_lds_dwordx4 v220, s[46:47] offset:0\nglobal_load_lds_dwordx4 v221, s[46:47] offset:1024\nglobal_load_lds_dwordx4 v222, s[46:47] offset:2048\n" \
  "global_load_lds_dwordx4 v223, s[46:47] offset:3072\ns_add_u32 s3, s30, 6\ns_and_b32 s3, s3, 7\ns_mul_i32 s3, s3, 196608\ns_mov_b32 m0, 94208\ns_add_u32 s46, s6, s3\n" \
  "s_addc_u32 s47, s7, 0\nglobal_load_lds_dwordx4 v216, s[46:47] offset:-4096\nglobal_load_lds_dwordx4 v217, s[46:47] offset:-3072\n" \
  "global_load_lds_dwordx4 v218, s[46:47] offset:-2048\nglobal_load_lds_dwordx4 v219, s[46:47] offset:-1024\nglobal_load_lds_dwordx4 v220, s[46:47] offset:0\n" \
  "global_load_lds_dwordx4 v221, s[46:47] offset:1024\nglobal_load_lds_dwordx4 v222, s[46:47] offset:2048\nglobal_load_lds_dwordx4 v223, s[46:47] offset:3072\n" \
  "s_add_u32 s3, s30, 7\ns_and_b32 s3, s3, 7\ns_mul_i32 s3, s3, 196608\ns_mov_b32 m0, 102400\ns_add_u32 s46, s6, s3\ns_addc_u32 s47, s7, 0\n" \
  "global_load_lds_dwordx4 v216, s[46:47] offset:-4096\nglobal_load_lds_dwordx4 v217, s[46:47] offset:-3072\nglobal_load_lds_dwordx4 v218, s[46:47] offset:-2048\n" \
  "global_load_lds_dwordx4 v219, s[46:47] offset:-1024\nglobal_load_lds_dwordx4 v220, s[46:47] offset:0\nglobal_load_lds_dwordx4 v221, s[46:47] offset:1024\n" \
  "global_load_lds_dwordx4 v222, s[46:47] offset:2048\nglobal_load_lds_dwordx4 v223, s[46:47] offset:3072\ns_waitcnt vmcnt(32)\ns_barrier\ns_branch .Lfz_q\n.Lfz_setup:\n" \
  "s_and_b32 s33, s27, 3\ns_xor_b32 s0, s27, 4\ns_lshr_b32 s34, s0, 2\ns_sub_u32 s32, s28, 4\nv_lshrrev_b32 v7, 5, v1\nv_and_b32 v6, 31, v1\nv_lshlrev_b32 v7, 2, v7\n" \
  "v_sub_u32 v6, v6, v7\nv_mov_b32 v7, 0xff800000\nv_cmp_le_i32 s[40:41], 0, v6\nv_cmp_le_i32 s[42:43], 1, v6\nv_cmp_le_i32 s[44:45], 2, v6\n" \
  "v_cndmask_b32 v16, v7, 0, s[40:41]\nv_cndmask_b32 v17, v7, 0, s[42:43]\nv_cndmask_b32 v18, v7, 0, s[44:45]\nv_cmp_le_i32 s[40:41], 3, v6\nv_cmp_le_i32 s[42:43], 8, v6\n" \
  "v_cmp_le_i32 s[44:45], 9, v6\nv_cndmask_b32 v19, v7, 0, s[40:41]\nv_cndmask_b32 v20, v7, 0, s[42:43]\nv_cndmask_b32 v21, v7, 0, s[44:45]\nv_cmp_le_i32 s[40:41], 10, v6\n" \
  "v_cmp_le_i32 s[42:43], 11, v6\nv_cmp_le_i32 s[44:45], 16, v6\nv_cndmask_b32 v22, v7, 0, s[40:41]\nv_cndmask_b32 v23, v7, 0, s[42:43]\nv_cndmask_b32 v24, v7, 0, s[44:45]\n" \
  "v_cmp_le_i32 s[40:41], 17, v6\nv_cmp_le_i32 s[42:43], 18, v6\nv_cmp_le_i32 s[44:45], 19, v6\nv_cndmask_b32 v25, v7, 0, s[40:41]\nv_cndmask_b32 v26, v7, 0, s[42:43]\n" \
  "v_cndmask_b32 v27, v7, 0, s[44:45]\nv_cmp_le_i32 s[40:41], 24, v6\nv_cmp_le_i32 s[42:43], 25, v6\nv_cmp_le_i32 s[44:45], 26, v6\nv_cndmask_b32 v28, v7, 0, s[40:41]\n" \
  "v_cndmask_b32 v29, v7, 0, s[42:43]\nv_cndmask_b32 v30, v7, 0, s[44:45]\nv_cmp_le_i32 s[40:41], 27, v6\ns_nop 1\nv_cndmask_b32 v31, v7, 0, s[40:41]\n" \
  "v_mov_b32 v48, 0x3f803f80\nv_mov_b32 v49, v48\nv_mov_b64 v[50:51], v[48:49]\nv_mov_b64 v[52:53], v[48:49]\nv_mov_b64 v[54:55], v[48:49]\nv_mov_b64 v[56:57], v[48:49]\n" \
  "v_mov_b64 v[58:59], v[48:49]\nv_mov_b64 v[60:61], v[48:49]\nv_mov_b64 v[62:63], v[48:49]\nv_mov_b64 v[64:65], v[48:49]\nv_mov_b64 v[66:67], v[48:49]\n" \
  "v_mov_b64 v[68:69], 0\nv_mov_b64 v[70:71], 0\nv_mov_b64 v[72:73], 0\nv_mov_b64 v[74:75], 0\nv_lshrrev_b32 v128, 4, v1\nv_and_b32 v129, 1, v128\n" \
  "v_lshrrev_b32 v130, 1, v128\nv_and_b32 v131, 15, v1\nv_bfe_u32 v132, v1, 2, 2\ns_lshl_b32 s40, s33, 4\nv_add_u32 v133, s40, v131\nv_lshrrev_b32 v6, 3, v1\n" \
  "v_and_b32 v9, 7, v1\nv_lshlrev_b32 v8, 1, v128\nv_xor_b32 v9, v9, v8\nv_lshlrev_b32 v9, 4, v9\nv_lshl_or_b32 v10, v6, 10, v9\ns_lshl_b32 s40, s34, 12\n" \
  "s_lshl_b32 s41, s33, 9\ns_add_u32 s40, s40, s41\ns_add_u32 s40, s40, 16384\nv_lshlrev_b32 v6, 4, v131\nv_lshl_or_b32 v6, v130, 8, v6\nv_lshl_or_b32 v6, v129, 11, v6\n" \
  "v_add_u32 v11, s40, v6\ns_lshl_b32 s40, s0, 6\ns_add_u32 s40, s40, 25600\nv_lshl_add_u32 v84, v128, 4, s40\nv_and_b32 v7, 1, v1\nv_lshlrev_b32 v6, 2, v132\n" \
  "v_lshl_or_b32 v6, v128, 5, v6\nv_lshl_or_b32 v6, v7, 7, v6\ns_lshr_b32 s40, s33, 1\ns_lshl_b32 s40, s40, 8\ns_and_b32 s41, s33, 1\ns_lshl_b32 s41, s41, 4\n" \
  "s_add_u32 s40, s40, s41\ns_lshl_b32 s41, s33, 11\ns_sub_u32 s40, s40, s41\ns_add_u32 s40, s40, 9216\nv_add_u32 v85, s40, v6\nv_lshlrev_b32 v6, 2, v128\n" \
  "v_lshl_or_b32 v6, v132, 4, v6\nv_lshl_or_b32 v6, v7, 6, v6\ns_mul_i32 s40, s0, 1920\ns_sub_u32 s40, 24576, s40\nv_add_u32 v3, s40, v6\ns_mul_i32 s40, s33, 1792\n" \
  "s_sub_u32 s40, 9728, s40\nv_lshl_add_u32 v15, v131, 4, s40\ns_lshl_b32 s41, s34, 6\nv_lshlrev_b32 v6, 10, v133\nv_lshl_or_b32 v6, v129, 5, v6\n" \
  "v_lshl_or_b32 v6, v130, 4, v6\nv_or_b32 v12, s41, v6\ns_lshl_b32 s41, s0, 11\ns_add_u32 s41, s41, 81920\nv_add_u32 v14, s41, v2\nv_xor_b32 v13, 0x2000, v14\n" \
  "s_lshl_b32 s40, s27, 8\ns_add_u32 s40, s40, 157696\nv_lshl_add_u32 v86, v1, 2, s40\nv_lshlrev_b32 v7, 4, v130\nv_lshl_or_b32 v7, v129, 2, v7\nv_or_b32 v7, v7, v132\n" \
  "v_xor_b32 v9, s34, v129\nv_lshrrev_b32 v8, 1, v132\nv_lshl_or_b32 v9, v9, 1, v8\nv_and_b32 v8, 3, v1\nv_lshlrev_b32 v8, 3, v8\nv_lshl_or_b32 v7, v7, 7, v8\n" \
  "s_lshl_b32 s40, s34, 15\nv_lshl_or_b32 v7, v9, 5, v7\nv_or_b32 v76, s40, v7\nv_lshlrev_b32 v7, 2, v129\nv_cmp_eq_u32 vcc, v7, v131\nv_xor_b32 v77, 32, v76\n" \
  "v_xor_b32 v78, 64, v76\nv_xor_b32 v79, 96, v76\nv_cndmask_b32 v228, 0, v48, vcc\nv_cndmask_b32 v229, 0, v48, vcc\nv_cndmask_b32 v230, 0, v48, vcc\n" \
  "v_cndmask_b32 v231, 0, v48, vcc\nv_and_b32 v6, 31, v1\nv_bfe_u32 v8, v1, 1, 3\nv_xor_b32 v8, v8, v130\nv_lshlrev_b32 v8, 4, v8\nv_lshl_or_b32 v8, v6, 7, v8\n" \
  "s_cmp_ge_u32 s27, 6\ns_cselect_b32 s40, 4096, 0\ns_cselect_b32 s42, 4160, 0\nv_xor_b32 v248, s42, v8\ns_mov_b32 s42, s40\ns_lshr_b32 s40, s40, 5\n" \
  "s_add_u32 s40, s40, 155904\nv_lshl_add_u32 v254, v6, 2, s40\nv_xor_b32 v249, 32, v248\nv_xor_b32 v250, 64, v248\nv_xor_b32 v251, 0x60, v248\n" \
  "v_lshl_or_b32 v6, v130, 3, v132\nv_lshlrev_b32 v6, 7, v6\nv_bfe_u32 v8, v1, 1, 1\nv_lshl_or_b32 v8, v129, 1, v8\nv_bfe_u32 v9, v1, 3, 1\ns_and_b32 s40, s27, 1\n" \
  "v_xor_b32 v9, s40, v9\nv_lshl_or_b32 v8, v9, 2, v8\nv_lshl_or_b32 v6, v8, 4, v6\nv_and_b32 v8, 1, v1\nv_lshl_or_b32 v6, v8, 3, v6\nv_or_b32 v6, s42, v6\n" \
  "v_add_u32 v252, 65536, v6\nv_add_u32 v253, 114688, v6\ns_mov_b32 s50, 0x33333333\ns_mov_b32 s51, 0x33333333\ns_lshl_b32 s40, s28, 3\ns_mul_i32 s40, s40, 27136\n" \
  "s_add_u32 s40, s40, 1048576\ns_mul_i32 s41, s29, 27136\ns_waitcnt lgkmcnt(0)\ns_add_u32 s40, s40, s41\ns_add_u32 s38, s24, s40\ns_addc_u32 s39, s25, 0\n" \
  "s_mul_i32 s41, s1, 217088\ns_lshl_b32 s40, s33, 11\ns_add_u32 s40, s40, s41\ns_add_u32 s40, s40, 1064960\ns_add_u32 s42, s24, s40\ns_addc_u32 s43, s25, 0\n" \
  "s_lshl_b32 s44, s34, 2\ns_add_u32 s44, s44, s31\ns_add_u32 s40, s44, 0\ns_and_b32 s40, s40, 7\ns_mul_i32 s40, s40, 27136\ns_add_u32 s60, s42, s40\n" \
  "s_addc_u32 s61, s43, 0\ns_add_u32 s40, s44, 1\ns_and_b32 s40, s40, 7\ns_mul_i32 s40, s40, 27136\ns_add_u32 s62, s42, s40\ns_addc_u32 s63, s43, 0\ns_add_u32 s40, s44, 2\n" \
  "s_and_b32 s40, s40, 7\ns_mul_i32 s40, s40, 27136\ns_add_u32 s64, s42, s40\ns_addc_u32 s65, s43, 0\ns_add_u32 s40, s44, 3\ns_and_b32 s40, s40, 7\n" \
  "s_mul_i32 s40, s40, 27136\ns_add_u32 s66, s42, s40\ns_addc_u32 s67, s43, 0\ns_lshl_b32 s40, s0, 11\ns_add_u32 s42, s38, s40\ns_addc_u32 s43, s39, 0\n" \
  "s_min_u32 s40, s28, 4\ns_mul_i32 s40, s40, 217088\ns_sub_u32 s52, s42, s40\ns_subb_u32 s53, s43, 0\ns_min_u32 s40, s28, 3\ns_mul_i32 s40, s40, 217088\n" \
  "s_sub_u32 s54, s42, s40\ns_subb_u32 s55, s43, 0\ns_min_u32 s40, s28, 2\ns_mul_i32 s40, s40, 217088\ns_sub_u32 s56, s42, s40\ns_subb_u32 s57, s43, 0\n" \
  "s_min_u32 s40, s28, 1\ns_mul_i32 s40, s40, 217088\ns_sub_u32 s58, s42, s40\ns_subb_u32 s59, s43, 0\ns_lshl_b32 s40, s2, 7\ns_lshl_b32 s41, s27, 13\n" \
  "s_add_u32 s41, s41, s40\ns_add_u32 s12, s12, s41\ns_addc_u32 s13, s13, 0\ns_lshl_b32 s41, s1, 16\ns_add_u32 s41, s41, s40\ns_add_u32 s22, s22, s41\n" \
  "s_addc_u32 s23, s23, 0\ns_lshl_b32 s40, s28, 3\ns_add_u32 s40, s40, s29\ns_lshl_b32 s41, s40, 1\ns_add_u32 s42, s40, 19\ns_cmp_lt_u32 s40, 24\ns_cselect_b32 s11, 1, 0\n" \
  "s_cmp_lt_u32 s40, 19\ns_cselect_b32 s11, 2, s11\ns_cselect_b32 s14, s41, s42\ns_lshl_b32 s41, s14, 15\ns_add_u32 s20, s20, s41\ns_addc_u32 s21, s21, 0\n" \
  "v_lshrrev_b32 v6, 2, v1\nv_xor_b32 v6, s33, v6\nv_and_b32 v7, 3, v1\nv_lshlrev_b32 v7, 4, v7\nv_lshl_or_b32 v87, v6, 6, v7\ns_cmp_gt_u32 s27, 3\n" \
  "s_cbranch_scc1 .Lfz_helper\nv_lshlrev_b32 v6, 4, v130\nv_add_u32 v100, 156160, v6\nv_mov_b32 v89, 0\nv_mov_b32 v90, 0\nv_mov_b32 v91, 0\nv_mov_b32 v93, 0\n" \
  "v_mov_b32 v94, 0\nv_mov_b32 v95, 0\nv_mov_b32 v97, 0\nv_mov_b32 v98, 0\nv_mov_b32 v99, 0\ns_and_b32 s40, s27, 1\ns_lshl_b32 s41, s40, 12\ns_add_u32 s41, s41, 4096\n" \
  "s_add_u32 s4, s38, s41\ns_addc_u32 s5, s39, 0\ns_lshl_b32 s40, s40, 8\ns_and_b32 s41, s27, 2\ns_lshl_b32 s41, s41, 5\ns_add_u32 s40, s40, s41\ns_add_u32 s40, s40, 24576\n" \
  "v_lshlrev_b32 v6, 4, v132\nv_lshl_or_b32 v6, v130, 9, v6\nv_lshl_or_b32 v6, v129, 7, v6\nv_add_u32 v103, s40, v6\ns_mov_b32 s6, 0x11111111\ns_mov_b32 s7, s6\n" \
  "s_and_b32 s40, s26, 7\ns_lshr_b32 s41, s26, 3\ns_mov_b32 s15, s40\ns_sub_u32 s42, s41, 10\ns_and_b32 s43, s40, 3\ns_cmp_lg_u32 s43, 1\ns_cbranch_scc1 .Lfz_a1\n" \
  "s_lshr_b32 s42, s41, 1\ns_cmp_eq_u32 s40, 1\ns_cselect_b32 s43, 4, 0\ns_sub_u32 s42, s42, s43\ns_bitcmp1_b32 s41, 0\ns_cselect_b32 s42, 99, s42\ns_branch .Lfz_a2\n" \
  ".Lfz_a1:\ns_cmp_ge_i32 s42, 0\ns_cbranch_scc1 .Lfz_a2\ns_add_u32 s43, s41, 12\ns_cmp_eq_u32 s40, 4\ns_cselect_b32 s42, s43, s42\ns_cselect_b32 s15, 1, s15\n" \
  "s_add_u32 s43, s41, 16\ns_cmp_eq_u32 s40, 6\ns_cselect_b32 s42, s43, s42\ns_cselect_b32 s15, 5, s15\n.Lfz_a2:\ns_cmp_lt_u32 s27, 2\n" \
  "s_cselect_b64 s[16:17], s[16:17], s[18:19]\ns_lshl_b32 s42, s42, 1\ns_and_b32 s43, s27, 1\ns_add_u32 s18, s42, s43\ns_min_u32 s41, s18, 42\ns_mul_i32 s40, s15, 176128\n" \
  "s_lshl_b32 s41, s41, 6\ns_add_u32 s40, s40, s41\ns_add_u32 s16, s16, s40\ns_addc_u32 s17, s17, 0\ns_lshl_b32 s43, s27, 12\ns_add_u32 s19, s43, 90112\n" \
  "v_lshrrev_b32 v6, 2, v1\nv_mul_u32_u24 v6, 2752, v6\nv_and_b32 v7, 3, v1\nv_lshl_add_u32 v120, v7, 4, v6\nv_add_u32 v121, 44032, v120\nv_add_u32 v122, 88064, v120\n" \
  "v_add_u32 v123, 132096, v120\ns_cmp_gt_u32 s27, 1\ns_cbranch_scc1 .Lfz_v\nv_mov_b32 v101, v2\nv_mov_b32 v6, 155648\ns_barrier\nds_read_b64 v[4:5], v6\n" \
  "ds_read_b64_tr_b16 v[136:137], v252 offset:0\nds_read_b64_tr_b16 v[138:139], v252 offset:512\nds_read_b128 v[128:131], v248 offset:0\n" \
  "ds_read_b128 v[132:135], v248 offset:4096\nds_read_b64_tr_b16 v[148:149], v252 offset:2048\nds_read_b64_tr_b16 v[150:151], v252 offset:2560\n" \
  "ds_read_b128 v[140:143], v249 offset:0\nds_read_b128 v[144:147], v249 offset:4096\ns_waitcnt lgkmcnt(4)\nv_mfma_f32_32x32x16_bf16 v[192:207], v[136:139], v[128:131], 0\n" \
  "ds_read_b64_tr_b16 v[160:161], v252 offset:4096\nds_read_b64_tr_b16 v[162:163], v252 offset:4608\nv_mfma_f32_32x32x16_bf16 v[208:223], v[136:139], v[132:135], 0\n" \
  "ds_read_b128 v[152:155], v250 offset:0\nds_read_b128 v[156:159], v250 offset:4096\nv_readfirstlane_b32 s48, v4\nv_readfirstlane_b32 s49, v5\ns_add_u32 s48, s48, 1\n" \
  "s_addc_u32 s49, s49, 0\ns_mul_i32 s35, s48, 0x9e3779b1\ns_mul_i32 s36, s49, 0x85ebca77\ns_add_u32 s35, s35, s36\ns_lshl_b32 s36, s35, 1\ns_waitcnt lgkmcnt(4)\n" \
  "v_mfma_f32_32x32x16_bf16 v[192:207], v[148:151], v[140:143], v[192:207]\nds_read_b64_tr_b16 v[172:173], v252 offset:6144\n" \
  "ds_read_b64_tr_b16 v[174:175], v252 offset:6656\nv_mfma_f32_32x32x16_bf16 v[208:223], v[148:151], v[144:147], v[208:223]\nds_read_b128 v[164:167], v251 offset:0\n" \
  "ds_read_b128 v[168:171], v251 offset:4096\nv_mfma_f32_16x16x32_bf16 v[224:227], v[228:231], v[136:139], 0\n" \
  "v_mfma_f32_16x16x32_bf16 v[224:227], v[228:231], v[148:151], v[224:227]\ns_waitcnt lgkmcnt(4)\nv_mfma_f32_32x32x16_bf16 v[192:207], v[160:163], v[152:155], v[192:207]\n" \
  "s_barrier\nds_read_b64_tr_b16 v[136:137], v252 offset:16384\nds_read_b64_tr_b16 v[138:139], v252 offset:16896\n" \
  "v_mfma_f32_32x32x16_bf16 v[208:223], v[160:163], v[156:159], v[208:223]\nds_read_b128 v[128:131], v248 offset:8192\nds_read_b128 v[132:135], v248 offset:12288\n" \
  "s_waitcnt lgkmcnt(4)\nv_mfma_f32_32x32x16_bf16 v[192:207], v[172:175], v[164:167], v[192:207]\nds_read_b64_tr_b16 v[148:149], v252 offset:18432\n" \
  "ds_read_b64_tr_b16 v[150:151], v252 offset:18944\nv_mfma_f32_32x32x16_bf16 v[208:223], v[172:175], v[168:171], v[208:223]\nds_read_b128 v[140:143], v249 offset:8192\n" \
  "ds_read_b128 v[144:147], v249 offset:12288\nv_mfma_f32_16x16x32_bf16 v[224:227], v[228:231], v[160:163], v[224:227]\n" \
  "v_mfma_f32_16x16x32_bf16 v[224:227], v[228:231], v[172:175], v[224:227]\ns_waitcnt lgkmcnt(4)\nv_mfma_f32_32x32x16_bf16 v[192:207], v[136:139], v[128:131], v[192:207]\n" \
  "ds_read_b64_tr_b16 v[160:161], v252 offset:20480\nds_read_b64_tr_b16 v[162:163], v252 offset:20992\n" \
  "v_mfma_f32_32x32x16_bf16 v[208:223], v[136:139], v[132:135], v[208:223]\nds_read_b128 v[152:155], v250 offset:8192\nds_read_b128 v[156:159], v250 offset:12288\n" \
  "s_waitcnt lgkmcnt(4)\nv_mfma_f32_32x32x16_bf16 v[192:207], v[148:151], v[140:143], v[192:207]\nds_read_b64_tr_b16 v[172:173], v252 offset:22528\n" \
  "ds_read_b64_tr_b16 v[174:175], v252 offset:23040\nv_mfma_f32_32x32x16_bf16 v[208:223], v[148:151], v[144:147], v[208:223]\nds_read_b128 v[164:167], v251 offset:8192\n" \
  "ds_read_b128 v[168:171], v251 offset:12288\nv_mfma_f32_16x16x32_bf16 v[224:227], v[228:231], v[136:139], v[224:227]\n" \
  "v_mfma_f32_16x16x32_bf16 v[224:227], v[228:231], v[148:151], v[224:227]\ns_waitcnt lgkmcnt(4)\nv_mfma_f32_32x32x16_bf16 v[192:207], v[160:163], v[152:155], v[192:207]\n" \
  "s_barrier\nds_read_b64_tr_b16 v[136:137], v252 offset:32768\nds_read_b64_tr_b16 v[138:139], v252 offset:33280\n" \
  "v_mfma_f32_32x32x16_bf16 v[208:223], v[160:163], v[156:159], v[208:223]\nds_read_b128 v[128:131], v248 offset:16384\nds_read_b128 v[132:135], v248 offset:20480\n" \
  "s_waitcnt lgkmcnt(4)\nv_mfma_f32_32x32x16_bf16 v[192:207], v[172:175], v[164:167], v[192:207]\nds_read_b64_tr_b16 v[148:149], v252 offset:34816\n" \
  "ds_read_b64_tr_b16 v[150:151], v252 offset:35328\nv_mfma_f32_32x32x16_bf16 v[208:223], v[172:175], v[168:171], v[208:223]\nds_read_b128 v[140:143], v249 offset:16384\n" \
  "ds_read_b128 v[144:147], v249 offset:20480\nv_mfma_f32_16x16x32_bf16 v[224:227], v[228:231], v[160:163], v[224:227]\n" \
  "v_mfma_f32_16x16x32_bf16 v[224:227], v[228:231], v[172:175], v[224:227]\ns_waitcnt lgkmcnt(4)\nv_mfma_f32_32x32x16_bf16 v[192:207], v[136:139], v[128:131], v[192:207]\n" \
  "ds_read_b64_tr_b16 v[160:161], v252 offset:36864\nds_read_b64_tr_b16 v[162:163], v252 offset:37376\n" \
  "v_mfma_f32_32x32x16_bf16 v[208:223], v[136:139], v[132:135], v[208:223]\nds_read_b128 v[152:155], v250 offset:16384\nds_read_b128 v[156:159], v250 offset:20480\n" \
  "s_waitcnt lgkmcnt(4)\nv_mfma_f32_32x32x16_bf16 v[192:207], v[148:151], v[140:143], v[192:207]\nds_read_b64_tr_b16 v[172:173], v252 offset:38912\n" \
  "ds_read_b64_tr_b16 v[174:175], v252 offset:39424\nv_mfma_f32_32x32x16_bf16 v[208:223], v[148:151], v[144:147], v[208:223]\nds_read_b128 v[164:167], v251 offset:16384\n" \
  "ds_read_b128 v[168:171], v251 offset:20480\nv_mfma_f32_16x16x32_bf16 v[224:227], v[228:231], v[136:139], v[224:227]\n" \
  "v_mfma_f32_16x16x32_bf16 v[224:227], v[228:231], v[148:151], v[224:227]\ns_waitcnt lgkmcnt(4)\nv_mfma_f32_32x32x16_bf16 v[192:207], v[160:163], v[152:155], v[192:207]\n" \
  "s_barrier\nds_read_b64_tr_b16 v[136:137], v253 offset:0\nds_read_b64_tr_b16 v[138:139], v253 offset:512\n" \
  "v_mfma_f32_32x32x16_bf16 v[208:223], v[160:163], v[156:159], v[208:223]\nds_read_b128 v[128:131], v248 offset:24576\nds_read_b128 v[132:135], v248 offset:28672\n" \
  "s_waitcnt lgkmcnt(4)\nv_mfma_f32_32x32x16_bf16 v[192:207], v[172:175], v[164:167], v[192:207]\nds_read_b64_tr_b16 v[148:149], v253 offset:2048\n" \
  "ds_read_b64_tr_b16 v[150:151], v253 offset:2560\nv_mfma_f32_32x32x16_bf16 v[208:223], v[172:175], v[168:171], v[208:223]\nds_read_b128 v[140:143], v249 offset:24576\n" \
  "ds_read_b128 v[144:147], v249 offset:28672\nv_mfma_f32_16x16x32_bf16 v[224:227], v[228:231], v[160:163], v[224:227]\n" \
  "v_mfma_f32_16x16x32_bf16 v[224:227], v[228:231], v[172:175], v[224:227]\ns_waitcnt lgkmcnt(4)\nv_mfma_f32_32x32x16_bf16 v[192:207], v[136:139], v[128:131], v[192:207]\n" \
  "ds_read_b64_tr_b16 v[160:161], v253 offset:4096\nds_read_b64_tr_b16 v[162:163], v253 offset:4608\n" \
  "v_mfma_f32_32x32x16_bf16 v[208:223], v[136:139], v[132:135], v[208:223]\nds_read_b128 v[152:155], v250 offset:24576\nds_read_b128 v[156:159], v250 offset:28672\n" \
  "s_waitcnt lgkmcnt(4)\nv_mfma_f32_32x32x16_bf16 v[192:207], v[148:151], v[140:143], v[192:207]\nds_read_b64_tr_b16 v[172:173], v253 offset:6144\n" \
  "ds_read_b64_tr_b16 v[174:175], v253 offset:6656\nv_mfma_f32_32x32x16_bf16 v[208:223], v[148:151], v[144:147], v[208:223]\nds_read_b128 v[164:167], v251 offset:24576\n" \
  "ds_read_b128 v[168:171], v251 offset:28672\nv_mfma_f32_16x16x32_bf16 v[224:227], v[228:231], v[136:139], v[224:227]\n" \
  "v_mfma_f32_16x16x32_bf16 v[224:227], v[228:231], v[148:151], v[224:227]\ns_waitcnt lgkmcnt(4)\nv_mfma_f32_32x32x16_bf16 v[192:207], v[160:163], v[152:155], v[192:207]\n" \
  "s_barrier\nds_read_b64_tr_b16 v[136:137], v252 offset:0\nds_read_b64_tr_b16 v[138:139], v252 offset:512\n" \
  "v_mfma_f32_32x32x16_bf16 v[208:223], v[160:163], v[156:159], v[208:223]\nds_read_b128 v[128:131], v248 offset:32768\nds_read_b128 v[132:135], v248 offset:36864\n" \
  "s_waitcnt lgkmcnt(4)\nv_mfma_f32_32x32x16_bf16 v[192:207], v[172:175], v[164:167], v[192:207]\nds_read_b64_tr_b16 v[148:149], v252 offset:2048\n" \
  "ds_read_b64_tr_b16 v[150:151], v252 offset:2560\nv_mfma_f32_32x32x16_bf16 v[208:223], v[172:175], v[168:171], v[208:223]\nds_read_b128 v[140:143], v249 offset:32768\n" \
  "ds_read_b128 v[144:147], v249 offset:36864\nv_mfma_f32_16x16x32_bf16 v[224:227], v[228:231], v[160:163], v[224:227]\n" \
  "v_mfma_f32_16x16x32_bf16 v[224:227], v[228:231], v[172:175], v[224:227]\ns_waitcnt lgkmcnt(4)\nv_mfma_f32_32x32x16_bf16 v[192:207], v[136:139], v[128:131], v[192:207]\n" \
  "ds_read_b64_tr_b16 v[160:161], v252 offset:4096\nds_read_b64_tr_b16 v[162:163], v252 offset:4608\n" \
  "v_mfma_f32_32x32x16_bf16 v[208:223], v[136:139], v[132:135], v[208:223]\nds_read_b128 v[152:155], v250 offset:32768\nds_read_b128 v[156:159], v250 offset:36864\n" \
  "s_waitcnt lgkmcnt(4)\nv_mfma_f32_32x32x16_bf16 v[192:207], v[148:151], v[140:143], v[192:207]\nds_read_b64_tr_b16 v[172:173], v252 offset:6144\n" \
  "ds_read_b64_tr_b16 v[174:175], v252 offset:6656\nv_mfma_f32_32x32x16_bf16 v[208:223], v[148:151], v[144:147], v[208:223]\nds_read_b128 v[164:167], v251 offset:32768\n" \
  "ds_read_b128 v[168:171], v251 offset:36864\nv_mfma_f32_16x16x32_bf16 v[224:227], v[228:231], v[136:139], v[224:227]\n" \
  "v_mfma_f32_16x16x32_bf16 v[224:227], v[228:231], v[148:151], v[224:227]\ns_waitcnt lgkmcnt(4)\nv_mfma_f32_32x32x16_bf16 v[192:207], v[160:163], v[152:155], v[192:207]\n" \
  "s_barrier\nds_read_b64_tr_b16 v[136:137], v252 offset:16384\nds_read_b64_tr_b16 v[138:139], v252 offset:16896\n" \
  "v_mfma_f32_32x32x16_bf16 v[208:223], v[160:163], v[156:159], v[208:223]\nds_read_b128 v[128:131], v248 offset:40960\nds_read_b128 v[132:135], v248 offset:45056\n" \
  "s_waitcnt lgkmcnt(4)\nv_mfma_f32_32x32x16_bf16 v[192:207], v[172:175], v[164:167], v[192:207]\nds_read_b64_tr_b16 v[148:149], v252 offset:18432\n" \
  "ds_read_b64_tr_b16 v[150:151], v252 offset:18944\nv_mfma_f32_32x32x16_bf16 v[208:223], v[172:175], v[168:171], v[208:223]\nds_read_b128 v[140:143], v249 offset:40960\n" \
  "ds_read_b128 v[144:147], v249 offset:45056\nv_mfma_f32_16x16x32_bf16 v[224:227], v[228:231], v[160:163], v[224:227]\n" \
  "v_mfma_f32_16x16x32_bf16 v[224:227], v[228:231], v[172:175], v[224:227]\ns_waitcnt lgkmcnt(4)\nv_mfma_f32_32x32x16_bf16 v[192:207], v[136:139], v[128:131], v[192:207]\n" \
  "ds_read_b64_tr_b16 v[160:161], v252 offset:20480\nds_read_b64_tr_b16 v[162:163], v252 offset:20992\n" \
  "v_mfma_f32_32x32x16_bf16 v[208:223], v[136:139], v[132:135], v[208:223]\nds_read_b128 v[152:155], v250 offset:40960\nds_read_b128 v[156:159], v250 offset:45056\n" \
  "s_waitcnt lgkmcnt(4)\nv_mfma_f32_32x32x16_bf16 v[192:207], v[148:151], v[140:143], v[192:207]\nds_read_b64_tr_b16 v[172:173], v252 offset:22528\n" \
  "ds_read_b64_tr_b16 v[174:175], v252 offset:23040\nv_mfma_f32_32x32x16_bf16 v[208:223], v[148:151], v[144:147], v[208:223]\nds_read_b128 v[164:167], v251 offset:40960\n" \
  "ds_read_b128 v[168:171], v251 offset:45056\nv_mfma_f32_16x16x32_bf16 v[224:227], v[228:231], v[136:139], v[224:227]\n" \
  "v_mfma_f32_16x16x32_bf16 v[224:227], v[228:231], v[148:151], v[224:227]\ns_waitcnt lgkmcnt(4)\nv_mfma_f32_32x32x16_bf16 v[192:207], v[160:163], v[152:155], v[192:207]\n" \
  "s_barrier\nds_read_b64_tr_b16 v[136:137], v252 offset:32768\nds_read_b64_tr_b16 v[138:139], v252 offset:33280\n" \
  "v_mfma_f32_32x32x16_bf16 v[208:223], v[160:163], v[156:159], v[208:223]\nds_read_b128 v[128:131], v248 offset:49152\nds_read_b128 v[132:135], v248 offset:53248\n" \
  "s_waitcnt lgkmcnt(4)\nv_mfma_f32_32x32x16_bf16 v[192:207], v[172:175], v[164:167], v[192:207]\nds_read_b64_tr_b16 v[148:149], v252 offset:34816\n" \
  "ds_read_b64_tr_b16 v[150:151], v252 offset:35328\nv_mfma_f32_32x32x16_bf16 v[208:223], v[172:175], v[168:171], v[208:223]\nds_read_b128 v[140:143], v249 offset:49152\n" \
  "ds_read_b128 v[144:147], v249 offset:53248\nv_mfma_f32_16x16x32_bf16 v[224:227], v[228:231], v[160:163], v[224:227]\n" \
  "v_mfma_f32_16x16x32_bf16 v[224:227], v[228:231], v[172:175], v[224:227]\ns_waitcnt lgkmcnt(4)\nv_mfma_f32_32x32x16_bf16 v[192:207], v[136:139], v[128:131], v[192:207]\n" \
  "ds_read_b64_tr_b16 v[160:161], v252 offset:36864\nds_read_b64_tr_b16 v[162:163], v252 offset:37376\n" \
  "v_mfma_f32_32x32x16_bf16 v[208:223], v[136:139], v[132:135], v[208:223]\nds_read_b128 v[152:155], v250 offset:49152\nds_read_b128 v[156:159], v250 offset:53248\n" \
  "s_waitcnt lgkmcnt(4)\nv_mfma_f32_32x32x16_bf16 v[192:207], v[148:151], v[140:143], v[192:207]\nds_read_b64_tr_b16 v[172:173], v252 offset:38912\n" \
  "ds_read_b64_tr_b16 v[174:175], v252 offset:39424\nv_mfma_f32_32x32x16_bf16 v[208:223], v[148:151], v[144:147], v[208:223]\nds_read_b128 v[164:167], v251 offset:49152\n" \
  "ds_read_b128 v[168:171], v251 offset:53248\nv_mfma_f32_16x16x32_bf16 v[224:227], v[228:231], v[136:139], v[224:227]\n" \
  "v_mfma_f32_16x16x32_bf16 v[224:227], v[228:231], v[148:151], v[224:227]\ns_waitcnt lgkmcnt(4)\nv_mfma_f32_32x32x16_bf16 v[192:207], v[160:163], v[152:155], v[192:207]\n" \
  "s_waitcnt lgkmcnt(0)\ns_barrier\nds_read_b64_tr_b16 v[136:137], v253 offset:0\nds_read_b64_tr_b16 v[138:139], v253 offset:512\n" \
  "v_mfma_f32_32x32x16_bf16 v[208:223], v[160:163], v[156:159], v[208:223]\nds_read_b128 v[128:131], v248 offset:57344\nds_read_b128 v[132:135], v248 offset:61440\n" \
  "s_waitcnt lgkmcnt(4)\nv_mfma_f32_32x32x16_bf16 v[192:207], v[172:175], v[164:167], v[192:207]\nds_read_b64_tr_b16 v[148:149], v253 offset:2048\n" \
  "ds_read_b64_tr_b16 v[150:151], v253 offset:2560\nv_mfma_f32_32x32x16_bf16 v[208:223], v[172:175], v[168:171], v[208:223]\nds_read_b128 v[140:143], v249 offset:57344\n" \
  "ds_read_b128 v[144:147], v249 offset:61440\nv_mfma_f32_16x16x32_bf16 v[224:227], v[228:231], v[160:163], v[224:227]\n" \
  "v_mfma_f32_16x16x32_bf16 v[224:227], v[228:231], v[172:175], v[224:227]\ns_waitcnt lgkmcnt(4)\nv_mfma_f32_32x32x16_bf16 v[192:207], v[136:139], v[128:131], v[192:207]\n" \
  "ds_read_b64_tr_b16 v[160:161], v253 offset:4096\nds_read_b64_tr_b16 v[162:163], v253 offset:4608\n" \
  "v_mfma_f32_32x32x16_bf16 v[208:223], v[136:139], v[132:135], v[208:223]\nds_read_b128 v[152:155], v250 offset:57344\nds_read_b128 v[156:159], v250 offset:61440\n" \
  "s_waitcnt lgkmcnt(4)\nv_mfma_f32_32x32x16_bf16 v[192:207], v[148:151], v[140:143], v[192:207]\nds_read_b64_tr_b16 v[172:173], v253 offset:6144\n" \
  "ds_read_b64_tr_b16 v[174:175], v253 offset:6656\nv_mfma_f32_32x32x16_bf16 v[208:223], v[148:151], v[144:147], v[208:223]\nds_read_b128 v[164:167], v251 offset:57344\n" \
  "ds_read_b128 v[168:171], v251 offset:61440\ns_barrier\nds_read_b32 v88, v254\nds_read_b32 v92, v254 offset:128\n" \
  "v_mfma_f32_16x16x32_bf16 v[224:227], v[228:231], v[136:139], v[224:227]\nv_mfma_f32_16x16x32_bf16 v[224:227], v[228:231], v[148:151], v[224:227]\ns_waitcnt lgkmcnt(6)\n" \
  "v_mfma_f32_16x16x32_bf16 v[224:227], v[228:231], v[160:163], v[224:227]\nds_read_b32 v128, v254 offset:256\nds_read_b32 v132, v254 offset:384\n" \
  "v_mfma_f32_32x32x16_bf16 v[192:207], v[160:163], v[152:155], v[192:207]\ns_waitcnt lgkmcnt(6)\nv_mfma_f32_16x16x32_bf16 v[224:227], v[228:231], v[172:175], v[224:227]\n" \
  "v_mfma_f32_32x32x16_bf16 v[208:223], v[160:163], v[156:159], v[208:223]\ns_waitcnt lgkmcnt(2)\ns_nop 4\n" \
  "v_mfma_f32_32x32x16_bf16 v[192:207], v[172:175], v[164:167], v[192:207]\nv_cvt_pk_bf16_f32 v6, v224, v224\nv_and_b32 v6, 0xffff0000, v6\nv_sub_f32 v7, v224, v6\n" \
  "v_cvt_pk_bf16_f32 v96, v6, v7\nv_mfma_f32_32x32x16_bf16 v[208:223], v[172:175], v[168:171], v[208:223]\ns_nop 0\n" \
  "v_mfma_f32_32x32x16_bf16 v[192:207], v[96:99], v[88:91], v[192:207]\nv_mfma_f32_32x32x16_bf16 v[208:223], v[96:99], v[92:95], v[208:223]\ns_waitcnt lgkmcnt(0)\ns_nop 9\n" \
  "v_mul_f32 v192, v192, v128\nv_mul_f32 v193, v193, v128\nv_mul_f32 v194, v194, v128\nv_mul_f32 v195, v195, v128\nv_mul_f32 v196, v196, v128\nv_mul_f32 v197, v197, v128\n" \
  "v_mul_f32 v198, v198, v128\nv_mul_f32 v199, v199, v128\nv_cvt_pk_bf16_f32 v232, v192, v193\nv_cvt_pk_bf16_f32 v233, v194, v195\nv_cvt_pk_bf16_f32 v234, v196, v197\n" \
  "v_cvt_pk_bf16_f32 v235, v198, v199\nglobal_store_dwordx4 v101, v[232:235], s[4:5] offset:-4096 sc1\nv_mul_f32 v200, v200, v128\nv_mul_f32 v201, v201, v128\n" \
  "v_mul_f32 v202, v202, v128\nv_mul_f32 v203, v203, v128\nv_mul_f32 v204, v204, v128\nv_mul_f32 v205, v205, v128\nv_mul_f32 v206, v206, v128\nv_mul_f32 v207, v207, v128\n" \
  "v_cvt_pk_bf16_f32 v236, v200, v201\nv_cvt_pk_bf16_f32 v237, v202, v203\nv_cvt_pk_bf16_f32 v238, v204, v205\nv_cvt_pk_bf16_f32 v239, v206, v207\n" \
  "global_store_dwordx4 v101, v[236:239], s[4:5] offset:-2048 sc1\nv_mul_f32 v208, v208, v132\nv_mul_f32 v209, v209, v132\nv_mul_f32 v210, v210, v132\n" \
  "v_mul_f32 v211, v211, v132\nv_mul_f32 v212, v212, v132\nv_mul_f32 v213, v213, v132\nv_mul_f32 v214, v214, v132\nv_mul_f32 v215, v215, v132\n" \
  "v_cvt_pk_bf16_f32 v240, v208, v209\nv_cvt_pk_bf16_f32 v241, v210, v211\nv_cvt_pk_bf16_f32 v242, v212, v213\nv_cvt_pk_bf16_f32 v243, v214, v215\ns_add_u32 s4, s4, 8192\n" \
  "s_addc_u32 s5, s5, 0\nglobal_store_dwordx4 v101, v[240:243], s[4:5] offset:-4096 sc1\nv_mul_f32 v216, v216, v132\nv_mul_f32 v217, v217, v132\nv_mul_f32 v218, v218, v132\n" \
  "v_mul_f32 v219, v219, v132\nv_mul_f32 v220, v220, v132\nv_mul_f32 v221, v221, v132\nv_mul_f32 v222, v222, v132\nv_mul_f32 v223, v223, v132\n" \
  "v_cvt_pk_bf16_f32 v244, v216, v217\nv_cvt_pk_bf16_f32 v245, v218, v219\nv_cvt_pk_bf16_f32 v246, v220, v221\nv_cvt_pk_bf16_f32 v247, v222, v223\n" \
  "global_store_dwordx4 v101, v[244:247], s[4:5] offset:-2048 sc1\nv_add3_u32 v192, v232, v233, v234\nv_add3_u32 v193, v236, v237, v238\nv_add3_u32 v194, v240, v241, v242\n" \
  "v_add3_u32 v195, v244, v245, v246\nv_add_u32 v192, v192, v235\nv_add_u32 v193, v193, v239\nv_add_u32 v194, v194, v243\nv_add_u32 v195, v195, v247\n" \
  "v_add_u32_dpp v192, v192, v192 quad_perm:[1,0,3,2] row_mask:0xf bank_mask:0xf\nv_add_u32_dpp v193, v193, v193 quad_perm:[1,0,3,2] row_mask:0xf bank_mask:0xf\n" \
  "v_add_u32_dpp v194, v194, v194 quad_perm:[1,0,3,2] row_mask:0xf bank_mask:0xf\nv_add_u32_dpp v195, v195, v195 quad_perm:[1,0,3,2] row_mask:0xf bank_mask:0xf\n" \
  "v_add_u32_dpp v192, v192, v192 quad_perm:[2,3,0,1] row_mask:0xf bank_mask:0xf\nv_add_u32_dpp v193, v193, v193 quad_perm:[2,3,0,1] row_mask:0xf bank_mask:0xf\n" \
  "v_add_u32_dpp v194, v194, v194 quad_perm:[2,3,0,1] row_mask:0xf bank_mask:0xf\nv_add_u32_dpp v195, v195, v195 quad_perm:[2,3,0,1] row_mask:0xf bank_mask:0xf\n" \
  "v_sub_u32 v192, s35, v192\nv_sub_u32 v193, s35, v193\nv_sub_u32 v194, s35, v194\nv_sub_u32 v195, s35, v195\ns_and_b32 s40, s27, 1\nv_permlane32_swap_b32 v192, v194\n" \
  "v_permlane32_swap_b32 v193, v195\ns_lshl_b32 s40, s40, 12\ns_add_u32 s40, s40, 122880\nv_permlane16_swap_b32 v192, v193\nv_permlane16_swap_b32 v194, v195\n" \
  "s_mov_b64 exec, s[6:7]\nglobal_store_dwordx4 v103, v[192:195], s[38:39] sc1\ns_mov_b64 exec, -1\nv_add_u32 v101, s40, v101\ns_barrier\n" \
  "ds_write_b128 v101, v[232:235] offset:0\nds_write_b128 v101, v[236:239] offset:2048\nds_write_b128 v101, v[240:243] offset:8192\n" \
  "ds_write_b128 v101, v[244:247] offset:10240\ns_branch .Lfz_kv2\n.Lfz_v:\nv_lshlrev_b32 v6, 4, v131\nv_lshl_or_b32 v6, v130, 9, v6\nv_lshl_or_b32 v6, v129, 11, v6\n" \
  "v_add_u32 v101, 1024, v6\nv_mov_b32 v6, 155648\ns_barrier\nds_read_b64 v[4:5], v6\nds_read_b64_tr_b16 v[136:137], v252 offset:8192\n" \
  "ds_read_b64_tr_b16 v[138:139], v252 offset:8704\nds_read_b128 v[128:131], v248 offset:0\nds_read_b128 v[132:135], v248 offset:4096\n" \
  "ds_read_b64_tr_b16 v[148:149], v252 offset:10240\nds_read_b64_tr_b16 v[150:151], v252 offset:10752\nds_read_b128 v[140:143], v249 offset:0\n" \
  "ds_read_b128 v[144:147], v249 offset:4096\ns_waitcnt lgkmcnt(4)\nv_mfma_f32_32x32x16_bf16 v[192:207], v[128:131], v[136:139], 0\n" \
  "ds_read_b64_tr_b16 v[160:161], v252 offset:12288\nds_read_b64_tr_b16 v[162:163], v252 offset:12800\nv_mfma_f32_32x32x16_bf16 v[208:223], v[132:135], v[136:139], 0\n" \
  "ds_read_b128 v[152:155], v250 offset:0\nds_read_b128 v[156:159], v250 offset:4096\nv_readfirstlane_b32 s48, v4\nv_readfirstlane_b32 s49, v5\ns_add_u32 s48, s48, 1\n" \
  "s_addc_u32 s49, s49, 0\ns_mul_i32 s35, s48, 0x9e3779b1\ns_mul_i32 s36, s49, 0x85ebca77\ns_add_u32 s35, s35, s36\ns_lshl_b32 s36, s35, 1\ns_waitcnt lgkmcnt(4)\n" \
  "v_mfma_f32_32x32x16_bf16 v[192:207], v[140:143], v[148:151], v[192:207]\nds_read_b64_tr_b16 v[172:173], v252 offset:14336\n" \
  "ds_read_b64_tr_b16 v[174:175], v252 offset:14848\nv_mfma_f32_32x32x16_bf16 v[208:223], v[144:147], v[148:151], v[208:223]\nds_read_b128 v[164:167], v251 offset:0\n" \
  "ds_read_b128 v[168:171], v251 offset:4096\nv_mfma_f32_16x16x32_bf16 v[224:227], v[228:231], v[136:139], 0\n" \
  "v_mfma_f32_16x16x32_bf16 v[224:227], v[228:231], v[148:151], v[224:227]\ns_waitcnt lgkmcnt(4)\nv_mfma_f32_32x32x16_bf16 v[192:207], v[152:155], v[160:163], v[192:207]\n" \
  "s_barrier\nds_read_b64_tr_b16 v[136:137], v252 offset:24576\nds_read_b64_tr_b16 v[138:139], v252 offset:25088\n" \
  "v_mfma_f32_32x32x16_bf16 v[208:223], v[156:159], v[160:163], v[208:223]\nds_read_b128 v[128:131], v248 offset:8192\nds_read_b128 v[132:135], v248 offset:12288\n" \
  "s_waitcnt lgkmcnt(4)\nv_mfma_f32_32x32x16_bf16 v[192:207], v[164:167], v[172:175], v[192:207]\nds_read_b64_tr_b16 v[148:149], v252 offset:26624\n" \
  "ds_read_b64_tr_b16 v[150:151], v252 offset:27136\nv_mfma_f32_32x32x16_bf16 v[208:223], v[168:171], v[172:175], v[208:223]\nds_read_b128 v[140:143], v249 offset:8192\n" \
  "ds_read_b128 v[144:147], v249 offset:12288\nv_mfma_f32_16x16x32_bf16 v[224:227], v[228:231], v[160:163], v[224:227]\n" \
  "v_mfma_f32_16x16x32_bf16 v[224:227], v[228:231], v[172:175], v[224:227]\ns_waitcnt lgkmcnt(4)\nv_mfma_f32_32x32x16_bf16 v[192:207], v[128:131], v[136:139], v[192:207]\n" \
  "ds_read_b64_tr_b16 v[160:161], v252 offset:28672\nds_read_b64_tr_b16 v[162:163], v252 offset:29184\n" \
  "v_mfma_f32_32x32x16_bf16 v[208:223], v[132:135], v[136:139], v[208:223]\nds_read_b128 v[152:155], v250 offset:8192\nds_read_b128 v[156:159], v250 offset:12288\n" \
  "s_waitcnt lgkmcnt(4)\nv_mfma_f32_32x32x16_bf16 v[192:207], v[140:143], v[148:151], v[192:207]\nds_read_b64_tr_b16 v[172:173], v252 offset:30720\n" \
  "ds_read_b64_tr_b16 v[174:175], v252 offset:31232\nv_mfma_f32_32x32x16_bf16 v[208:223], v[144:147], v[148:151], v[208:223]\nds_read_b128 v[164:167], v251 offset:8192\n" \
  "ds_read_b128 v[168:171], v251 offset:12288\nv_mfma_f32_16x16x32_bf16 v[224:227], v[228:231], v[136:139], v[224:227]\n" \
  "v_mfma_f32_16x16x32_bf16 v[224:227], v[228:231], v[148:151], v[224:227]\ns_waitcnt lgkmcnt(4)\nv_mfma_f32_32x32x16_bf16 v[192:207], v[152:155], v[160:163], v[192:207]\n" \
  "s_barrier\nds_read_b64_tr_b16 v[136:137], v252 offset:40960\nds_read_b64_tr_b16 v[138:139], v252 offset:41472\n" \
  "v_mfma_f32_32x32x16_bf16 v[208:223], v[156:159], v[160:163], v[208:223]\nds_read_b128 v[128:131], v248 offset:16384\nds_read_b128 v[132:135], v248 offset:20480\n" \
  "s_waitcnt lgkmcnt(4)\nv_mfma_f32_32x32x16_bf16 v[192:207], v[164:167], v[172:175], v[192:207]\nds_read_b64_tr_b16 v[148:149], v252 offset:43008\n" \
  "ds_read_b64_tr_b16 v[150:151], v252 offset:43520\nv_mfma_f32_32x32x16_bf16 v[208:223], v[168:171], v[172:175], v[208:223]\nds_read_b128 v[140:143], v249 offset:16384\n" \
  "ds_read_b128 v[144:147], v249 offset:20480\nv_mfma_f32_16x16x32_bf16 v[224:227], v[228:231], v[160:163], v[224:227]\n" \
  "v_mfma_f32_16x16x32_bf16 v[224:227], v[228:231], v[172:175], v[224:227]\ns_waitcnt lgkmcnt(4)\nv_mfma_f32_32x32x16_bf16 v[192:207], v[128:131], v[136:139], v[192:207]\n" \
  "ds_read_b64_tr_b16 v[160:161], v252 offset:45056\nds_read_b64_tr_b16 v[162:163], v252 offset:45568\n" \
  "v_mfma_f32_32x32x16_bf16 v[208:223], v[132:135], v[136:139], v[208:223]\nds_read_b128 v[152:155], v250 offset:16384\nds_read_b128 v[156:159], v250 offset:20480\n" \
  "s_waitcnt lgkmcnt(4)\nv_mfma_f32_32x32x16_bf16 v[192:207], v[140:143], v[148:151], v[192:207]\nds_read_b64_tr_b16 v[172:173], v252 offset:47104\n" \
  "ds_read_b64_tr_b16 v[174:175], v252 offset:47616\nv_mfma_f32_32x32x16_bf16 v[208:223], v[144:147], v[148:151], v[208:223]\nds_read_b128 v[164:167], v251 offset:16384\n" \
  "ds_read_b128 v[168:171], v251 offset:20480\nv_mfma_f32_16x16x32_bf16 v[224:227], v[228:231], v[136:139], v[224:227]\n" \
  "v_mfma_f32_16x16x32_bf16 v[224:227], v[228:231], v[148:151], v[224:227]\ns_waitcnt lgkmcnt(4)\nv_mfma_f32_32x32x16_bf16 v[192:207], v[152:155], v[160:163], v[192:207]\n" \
  "s_barrier\nds_read_b64_tr_b16 v[136:137], v253 offset:8192\nds_read_b64_tr_b16 v[138:139], v253 offset:8704\n" \
  "v_mfma_f32_32x32x16_bf16 v[208:223], v[156:159], v[160:163], v[208:223]\nds_read_b128 v[128:131], v248 offset:24576\nds_read_b128 v[132:135], v248 offset:28672\n" \
  "s_waitcnt lgkmcnt(4)\nv_mfma_f32_32x32x16_bf16 v[192:207], v[164:167], v[172:175], v[192:207]\nds_read_b64_tr_b16 v[148:149], v253 offset:10240\n" \
  "ds_read_b64_tr_b16 v[150:151], v253 offset:10752\nv_mfma_f32_32x32x16_bf16 v[208:223], v[168:171], v[172:175], v[208:223]\nds_read_b128 v[140:143], v249 offset:24576\n" \
  "ds_read_b128 v[144:147], v249 offset:28672\nv_mfma_f32_16x16x32_bf16 v[224:227], v[228:231], v[160:163], v[224:227]\n" \
  "v_mfma_f32_16x16x32_bf16 v[224:227], v[228:231], v[172:175], v[224:227]\ns_waitcnt lgkmcnt(4)\nv_mfma_f32_32x32x16_bf16 v[192:207], v[128:131], v[136:139], v[192:207]\n" \
  "ds_read_b64_tr_b16 v[160:161], v253 offset:12288\nds_read_b64_tr_b16 v[162:163], v253 offset:12800\n" \
  "v_mfma_f32_32x32x16_bf16 v[208:223], v[132:135], v[136:139], v[208:223]\nds_read_b128 v[152:155], v250 offset:24576\nds_read_b128 v[156:159], v250 offset:28672\n" \
  "s_waitcnt lgkmcnt(4)\nv_mfma_f32_32x32x16_bf16 v[192:207], v[140:143], v[148:151], v[192:207]\nds_read_b64_tr_b16 v[172:173], v253 offset:14336\n" \
  "ds_read_b64_tr_b16 v[174:175], v253 offset:14848\nv_mfma_f32_32x32x16_bf16 v[208:223], v[144:147], v[148:151], v[208:223]\nds_read_b128 v[164:167], v251 offset:24576\n" \
  "ds_read_b128 v[168:171], v251 offset:28672\nv_mfma_f32_16x16x32_bf16 v[224:227], v[228:231], v[136:139], v[224:227]\n" \
  "v_mfma_f32_16x16x32_bf16 v[224:227], v[228:231], v[148:151], v[224:227]\ns_waitcnt lgkmcnt(4)\nv_mfma_f32_32x32x16_bf16 v[192:207], v[152:155], v[160:163], v[192:207]\n" \
  "s_barrier\nds_read_b64_tr_b16 v[136:137], v252 offset:8192\nds_read_b64_tr_b16 v[138:139], v252 offset:8704\n" \
  "v_mfma_f32_32x32x16_bf16 v[208:223], v[156:159], v[160:163], v[208:223]\nds_read_b128 v[128:131], v248 offset:32768\nds_read_b128 v[132:135], v248 offset:36864\n" \
  "s_waitcnt lgkmcnt(4)\nv_mfma_f32_32x32x16_bf16 v[192:207], v[164:167], v[172:175], v[192:207]\nds_read_b64_tr_b16 v[148:149], v252 offset:10240\n" \
  "ds_read_b64_tr_b16 v[150:151], v252 offset:10752\nv_mfma_f32_32x32x16_bf16 v[208:223], v[168:171], v[172:175], v[208:223]\nds_read_b128 v[140:143], v249 offset:32768\n" \
  "ds_read_b128 v[144:147], v249 offset:36864\nv_mfma_f32_16x16x32_bf16 v[224:227], v[228:231], v[160:163], v[224:227]\n" \
  "v_mfma_f32_16x16x32_bf16 v[224:227], v[228:231], v[172:175], v[224:227]\ns_waitcnt lgkmcnt(4)\nv_mfma_f32_32x32x16_bf16 v[192:207], v[128:131], v[136:139], v[192:207]\n" \
  "ds_read_b64_tr_b16 v[160:161], v252 offset:12288\nds_read_b64_tr_b16 v[162:163], v252 offset:12800\n" \
  "v_mfma_f32_32x32x16_bf16 v[208:223], v[132:135], v[136:139], v[208:223]\nds_read_b128 v[152:155], v250 offset:32768\nds_read_b128 v[156:159], v250 offset:36864\n" \
  "s_waitcnt lgkmcnt(4)\nv_mfma_f32_32x32x16_bf16 v[192:207], v[140:143], v[148:151], v[192:207]\nds_read_b64_tr_b16 v[172:173], v252 offset:14336\n" \
  "ds_read_b64_tr_b16 v[174:175], v252 offset:14848\nv_mfma_f32_32x32x16_bf16 v[208:223], v[144:147], v[148:151], v[208:223]\nds_read_b128 v[164:167], v251 offset:32768\n" \
  "ds_read_b128 v[168:171], v251 offset:36864\nv_mfma_f32_16x16x32_bf16 v[224:227], v[228:231], v[136:139], v[224:227]\n" \
  "v_mfma_f32_16x16x32_bf16 v[224:227], v[228:231], v[148:151], v[224:227]\ns_waitcnt lgkmcnt(4)\nv_mfma_f32_32x32x16_bf16 v[192:207], v[152:155], v[160:163], v[192:207]\n" \
  "s_barrier\nds_read_b64_tr_b16 v[136:137], v252 offset:24576\nds_read_b64_tr_b16 v[138:139], v252 offset:25088\n" \
  "v_mfma_f32_32x32x16_bf16 v[208:223], v[156:159], v[160:163], v[208:223]\nds_read_b128 v[128:131], v248 offset:40960\nds_read_b128 v[132:135], v248 offset:45056\n" \
  "s_waitcnt lgkmcnt(4)\nv_mfma_f32_32x32x16_bf16 v[192:207], v[164:167], v[172:175], v[192:207]\nds_read_b64_tr_b16 v[148:149], v252 offset:26624\n" \
  "ds_read_b64_tr_b16 v[150:151], v252 offset:27136\nv_mfma_f32_32x32x16_bf16 v[208:223], v[168:171], v[172:175], v[208:223]\nds_read_b128 v[140:143], v249 offset:40960\n" \
  "ds_read_b128 v[144:147], v249 offset:45056\nv_mfma_f32_16x16x32_bf16 v[224:227], v[228:231], v[160:163], v[224:227]\n" \
  "v_mfma_f32_16x16x32_bf16 v[224:227], v[228:231], v[172:175], v[224:227]\ns_waitcnt lgkmcnt(4)\nv_mfma_f32_32x32x16_bf16 v[192:207], v[128:131], v[136:139], v[192:207]\n" \
  "ds_read_b64_tr_b16 v[160:161], v252 offset:28672\nds_read_b64_tr_b16 v[162:163], v252 offset:29184\n" \
  "v_mfma_f32_32x32x16_bf16 v[208:223], v[132:135], v[136:139], v[208:223]\nds_read_b128 v[152:155], v250 offset:40960\nds_read_b128 v[156:159], v250 offset:45056\n" \
  "s_waitcnt lgkmcnt(4)\nv_mfma_f32_32x32x16_bf16 v[192:207], v[140:143], v[148:151], v[192:207]\nds_read_b64_tr_b16 v[172:173], v252 offset:30720\n" \
  "ds_read_b64_tr_b16 v[174:175], v252 offset:31232\nv_mfma_f32_32x32x16_bf16 v[208:223], v[144:147], v[148:151], v[208:223]\nds_read_b128 v[164:167], v251 offset:40960\n" \
  "ds_read_b128 v[168:171], v251 offset:45056\nv_mfma_f32_16x16x32_bf16 v[224:227], v[228:231], v[136:139], v[224:227]\n" \
  "v_mfma_f32_16x16x32_bf16 v[224:227], v[228:231], v[148:151], v[224:227]\ns_waitcnt lgkmcnt(4)\nv_mfma_f32_32x32x16_bf16 v[192:207], v[152:155], v[160:163], v[192:207]\n" \
  "s_barrier\nds_read_b64_tr_b16 v[136:137], v252 offset:40960\nds_read_b64_tr_b16 v[138:139], v252 offset:41472\n" \
  "v_mfma_f32_32x32x16_bf16 v[208:223], v[156:159], v[160:163], v[208:223]\nds_read_b128 v[128:131], v248 offset:49152\nds_read_b128 v[132:135], v248 offset:53248\n" \
  "s_waitcnt lgkmcnt(4)\nv_mfma_f32_32x32x16_bf16 v[192:207], v[164:167], v[172:175], v[192:207]\nds_read_b64_tr_b16 v[148:149], v252 offset:43008\n" \
  "ds_read_b64_tr_b16 v[150:151], v252 offset:43520\nv_mfma_f32_32x32x16_bf16 v[208:223], v[168:171], v[172:175], v[208:223]\nds_read_b128 v[140:143], v249 offset:49152\n" \
  "ds_read_b128 v[144:147], v249 offset:53248\nv_mfma_f32_16x16x32_bf16 v[224:227], v[228:231], v[160:163], v[224:227]\n" \
  "v_mfma_f32_16x16x32_bf16 v[224:227], v[228:231], v[172:175], v[224:227]\ns_waitcnt lgkmcnt(4)\nv_mfma_f32_32x32x16_bf16 v[192:207], v[128:131], v[136:139], v[192:207]\n" \
  "ds_read_b64_tr_b16 v[160:161], v252 offset:45056\nds_read_b64_tr_b16 v[162:163], v252 offset:45568\n" \
  "v_mfma_f32_32x32x16_bf16 v[208:223], v[132:135], v[136:139], v[208:223]\nds_read_b128 v[152:155], v250 offset:49152\nds_read_b128 v[156:159], v250 offset:53248\n" \
  "s_waitcnt lgkmcnt(4)\nv_mfma_f32_32x32x16_bf16 v[192:207], v[140:143], v[148:151], v[192:207]\nds_read_b64_tr_b16 v[172:173], v252 offset:47104\n" \
  "ds_read_b64_tr_b16 v[174:175], v252 offset:47616\nv_mfma_f32_32x32x16_bf16 v[208:223], v[144:147], v[148:151], v[208:223]\nds_read_b128 v[164:167], v251 offset:49152\n" \
  "ds_read_b128 v[168:171], v251 offset:53248\nv_mfma_f32_16x16x32_bf16 v[224:227], v[228:231], v[136:139], v[224:227]\n" \
  "v_mfma_f32_16x16x32_bf16 v[224:227], v[228:231], v[148:151], v[224:227]\ns_waitcnt lgkmcnt(4)\nv_mfma_f32_32x32x16_bf16 v[192:207], v[152:155], v[160:163], v[192:207]\n" \
  "s_waitcnt lgkmcnt(0)\ns_barrier\nds_read_b64_tr_b16 v[136:137], v253 offset:8192\nds_read_b64_tr_b16 v[138:139], v253 offset:8704\n" \
  "v_mfma_f32_32x32x16_bf16 v[208:223], v[156:159], v[160:163], v[208:223]\nds_read_b128 v[128:131], v248 offset:57344\nds_read_b128 v[132:135], v248 offset:61440\n" \
  "s_waitcnt lgkmcnt(4)\nv_mfma_f32_32x32x16_bf16 v[192:207], v[164:167], v[172:175], v[192:207]\nds_read_b64_tr_b16 v[148:149], v253 offset:10240\n" \
  "ds_read_b64_tr_b16 v[150:151], v253 offset:10752\nv_mfma_f32_32x32x16_bf16 v[208:223], v[168:171], v[172:175], v[208:223]\nds_read_b128 v[140:143], v249 offset:57344\n" \
  "ds_read_b128 v[144:147], v249 offset:61440\nv_mfma_f32_16x16x32_bf16 v[224:227], v[228:231], v[160:163], v[224:227]\n" \
  "v_mfma_f32_16x16x32_bf16 v[224:227], v[228:231], v[172:175], v[224:227]\ns_waitcnt lgkmcnt(4)\nv_mfma_f32_32x32x16_bf16 v[192:207], v[128:131], v[136:139], v[192:207]\n" \
  "ds_read_b64_tr_b16 v[160:161], v253 offset:12288\nds_read_b64_tr_b16 v[162:163], v253 offset:12800\n" \
  "v_mfma_f32_32x32x16_bf16 v[208:223], v[132:135], v[136:139], v[208:223]\nds_read_b128 v[152:155], v250 offset:57344\nds_read_b128 v[156:159], v250 offset:61440\n" \
  "s_waitcnt lgkmcnt(4)\nv_mfma_f32_32x32x16_bf16 v[192:207], v[140:143], v[148:151], v[192:207]\nds_read_b64_tr_b16 v[172:173], v253 offset:14336\n" \
  "ds_read_b64_tr_b16 v[174:175], v253 offset:14848\nv_mfma_f32_32x32x16_bf16 v[208:223], v[144:147], v[148:151], v[208:223]\nds_read_b128 v[164:167], v251 offset:57344\n" \
  "ds_read_b128 v[168:171], v251 offset:61440\ns_barrier\nds_read_b32 v88, v254\nds_read_b32 v92, v254 offset:128\n" \
  "v_mfma_f32_16x16x32_bf16 v[224:227], v[228:231], v[136:139], v[224:227]\nv_mfma_f32_16x16x32_bf16 v[224:227], v[228:231], v[148:151], v[224:227]\ns_waitcnt lgkmcnt(6)\n" \
  "v_mfma_f32_16x16x32_bf16 v[224:227], v[228:231], v[160:163], v[224:227]\nds_read_b128 v[128:131], v100 offset:0\nds_read_b128 v[132:135], v100 offset:32\n" \
  "v_mfma_f32_32x32x16_bf16 v[192:207], v[152:155], v[160:163], v[192:207]\nds_read_b128 v[176:179], v100 offset:64\nds_read_b128 v[180:183], v100 offset:96\n" \
  "ds_read_b128 v[184:187], v100 offset:128\ns_waitcnt lgkmcnt(9)\nv_mfma_f32_16x16x32_bf16 v[224:227], v[228:231], v[172:175], v[224:227]\n" \
  "v_mfma_f32_32x32x16_bf16 v[208:223], v[156:159], v[160:163], v[208:223]\nds_read_b128 v[188:191], v100 offset:160\nds_read_b128 v[104:107], v100 offset:192\n" \
  "ds_read_b128 v[108:111], v100 offset:224\ns_waitcnt lgkmcnt(8)\ns_nop 1\nv_mfma_f32_32x32x16_bf16 v[192:207], v[164:167], v[172:175], v[192:207]\n" \
  "v_cvt_pk_bf16_f32 v6, v224, v224\nv_and_b32 v6, 0xffff0000, v6\nv_sub_f32 v7, v224, v6\nv_cvt_pk_bf16_f32 v96, v6, v7\n" \
  "v_mfma_f32_32x32x16_bf16 v[208:223], v[168:171], v[172:175], v[208:223]\ns_nop 0\nv_mfma_f32_32x32x16_bf16 v[192:207], v[88:91], v[96:99], v[192:207]\n" \
  "v_mfma_f32_32x32x16_bf16 v[208:223], v[92:95], v[96:99], v[208:223]\ns_waitcnt lgkmcnt(0)\ns_nop 9\nv_mul_f32 v192, v192, v128\nv_mul_f32 v193, v193, v129\n" \
  "v_mul_f32 v194, v194, v130\nv_mul_f32 v195, v195, v131\nv_mul_f32 v196, v196, v132\nv_mul_f32 v197, v197, v133\nv_mul_f32 v198, v198, v134\nv_mul_f32 v199, v199, v135\n" \
  "v_cvt_pk_bf16_f32 v232, v192, v193\nv_cvt_pk_bf16_f32 v233, v194, v195\nv_cvt_pk_bf16_f32 v234, v196, v197\nv_cvt_pk_bf16_f32 v235, v198, v199\n" \
  "global_store_dwordx4 v101, v[232:235], s[4:5] offset:-4096 sc1\nv_mul_f32 v200, v200, v176\nv_mul_f32 v201, v201, v177\nv_mul_f32 v202, v202, v178\n" \
  "v_mul_f32 v203, v203, v179\nv_mul_f32 v204, v204, v180\nv_mul_f32 v205, v205, v181\nv_mul_f32 v206, v206, v182\nv_mul_f32 v207, v207, v183\n" \
  "v_cvt_pk_bf16_f32 v236, v200, v201\nv_cvt_pk_bf16_f32 v237, v202, v203\nv_cvt_pk_bf16_f32 v238, v204, v205\nv_cvt_pk_bf16_f32 v239, v206, v207\n" \
  "global_store_dwordx4 v101, v[236:239], s[4:5] offset:-3840 sc1\nv_mul_f32 v208, v208, v184\nv_mul_f32 v209, v209, v185\nv_mul_f32 v210, v210, v186\n" \
  "v_mul_f32 v211, v211, v187\nv_mul_f32 v212, v212, v188\nv_mul_f32 v213, v213, v189\nv_mul_f32 v214, v214, v190\nv_mul_f32 v215, v215, v191\n" \
  "v_cvt_pk_bf16_f32 v240, v208, v209\nv_cvt_pk_bf16_f32 v241, v210, v211\nv_cvt_pk_bf16_f32 v242, v212, v213\nv_cvt_pk_bf16_f32 v243, v214, v215\ns_add_u32 s4, s4, 8192\n" \
  "s_addc_u32 s5, s5, 0\nglobal_store_dwordx4 v101, v[240:243], s[4:5] offset:-4096 sc1\nv_mul_f32 v216, v216, v104\nv_mul_f32 v217, v217, v105\nv_mul_f32 v218, v218, v106\n" \
  "v_mul_f32 v219, v219, v107\nv_mul_f32 v220, v220, v108\nv_mul_f32 v221, v221, v109\nv_mul_f32 v222, v222, v110\nv_mul_f32 v223, v223, v111\n" \
  "v_cvt_pk_bf16_f32 v244, v216, v217\nv_cvt_pk_bf16_f32 v245, v218, v219\nv_cvt_pk_bf16_f32 v246, v220, v221\nv_cvt_pk_bf16_f32 v247, v222, v223\n" \
  "global_store_dwordx4 v101, v[244:247], s[4:5] offset:-3840 sc1\nv_add3_u32 v192, v232, v233, v234\nv_add3_u32 v193, v236, v237, v238\nv_add3_u32 v194, v240, v241, v242\n" \
  "v_add3_u32 v195, v244, v245, v246\nv_add_u32 v192, v192, v235\nv_add_u32 v193, v193, v239\nv_add_u32 v194, v194, v243\nv_add_u32 v195, v195, v247\n" \
  "v_add_u32_dpp v192, v192, v192 quad_perm:[1,0,3,2] row_mask:0xf bank_mask:0xf\nv_add_u32_dpp v193, v193, v193 quad_perm:[1,0,3,2] row_mask:0xf bank_mask:0xf\n" \
  "v_add_u32_dpp v194, v194, v194 quad_perm:[1,0,3,2] row_mask:0xf bank_mask:0xf\nv_add_u32_dpp v195, v195, v195 quad_perm:[1,0,3,2] row_mask:0xf bank_mask:0xf\n" \
  "v_add_u32_dpp v192, v192, v192 quad_perm:[2,3,0,1] row_mask:0xf bank_mask:0xf\nv_add_u32_dpp v193, v193, v193 quad_perm:[2,3,0,1] row_mask:0xf bank_mask:0xf\n" \
  "v_add_u32_dpp v194, v194, v194 quad_perm:[2,3,0,1] row_mask:0xf bank_mask:0xf\nv_add_u32_dpp v195, v195, v195 quad_perm:[2,3,0,1] row_mask:0xf bank_mask:0xf\n" \
  "v_sub_u32 v192, s35, v192\nv_sub_u32 v193, s35, v193\nv_sub_u32 v194, s35, v194\nv_sub_u32 v195, s35, v195\ns_and_b32 s40, s27, 1\nv_permlane32_swap_b32 v192, v194\n" \
  "v_permlane32_swap_b32 v193, v195\ns_lshl_b32 s40, s40, 12\ns_add_u32 s40, s40, 122880\ns_mov_b64 exec, s[6:7]\nglobal_store_dwordx4 v103, v[192:195], s[38:39] sc1\n" \
  "s_mov_b64 exec, -1\nv_add_u32 v101, s40, v101\ns_barrier\nds_write_b128 v101, v[232:235] offset:0\nds_write_b128 v101, v[236:239] offset:256\n" \
  "ds_write_b128 v101, v[240:243] offset:8192\nds_write_b128 v101, v[244:247] offset:8448\n.Lfz_kv2:\ns_lshl_b32 s3, s27, 10\nv_mov_b64 v[248:249], v[120:121]\n" \
  "v_mov_b64 v[250:251], v[122:123]\nv_mov_b64 v[232:233], v[48:49]\nv_mov_b64 v[234:235], v[48:49]\nv_mov_b64 v[236:237], v[48:49]\nv_mov_b64 v[238:239], v[48:49]\n" \
  "v_mov_b64 v[240:241], v[48:49]\nv_mov_b64 v[242:243], v[48:49]\nv_mov_b64 v[244:245], v[48:49]\nv_mov_b64 v[246:247], v[48:49]\ns_waitcnt vmcnt(63)\ns_sleep 8\n" \
  "s_cmp_ge_i32 s32, -3\ns_cbranch_scc0 .Lfz_dm1\nglobal_load_dwordx4 v[100:103], v2, s[58:59] sc1\nglobal_load_dwordx4 v[116:119], v2, s[58:59] offset:1024 sc1\n" \
  "s_mov_b64 exec, s[50:51]\nglobal_load_dword v71, v3, s[58:59] sc1\n.Lfz_dr1:\ns_mov_b64 exec, -1\ns_cmp_ge_i32 s32, -2\ns_cbranch_scc0 .Lfz_dm2\n" \
  "global_load_dwordx4 v[96:99], v2, s[56:57] sc1\nglobal_load_dwordx4 v[112:115], v2, s[56:57] offset:1024 sc1\ns_mov_b64 exec, s[50:51]\n" \
  "global_load_dword v70, v3, s[56:57] sc1\n.Lfz_dr2:\ns_mov_b64 exec, -1\ns_barrier\ns_cmp_ge_i32 s32, -1\ns_cbranch_scc0 .Lfz_dm3\n" \
  "global_load_dwordx4 v[92:95], v2, s[54:55] sc1\nglobal_load_dwordx4 v[108:111], v2, s[54:55] offset:1024 sc1\ns_mov_b64 exec, s[50:51]\n" \
  "global_load_dword v69, v3, s[54:55] sc1\n.Lfz_dr3:\ns_mov_b64 exec, -1\ns_cmp_ge_i32 s32, 0\ns_cbranch_scc0 .Lfz_dm4\nglobal_load_dwordx4 v[88:91], v2, s[52:53] sc1\n" \
  "global_load_dwordx4 v[104:107], v2, s[52:53] offset:1024 sc1\ns_mov_b64 exec, s[50:51]\nglobal_load_dword v68, v3, s[52:53] sc1\n.Lfz_dr4:\ns_mov_b64 exec, -1\n" \
  "s_add_u32 s46, s24, 4096\ns_addc_u32 s47, s25, 0\nglobal_load_dwordx4 v[224:227], v2, s[46:47] sc1\nglobal_load_dwordx4 v[228:231], v2, s[46:47] offset:1024 sc1\n" \
  "s_branch .Lfz_attn\n.Lfz_helper:\ns_mul_i32 s40, s27, 3\ns_sub_u32 s40, s40, 13\ns_cmp_eq_u32 s27, 4\ns_cselect_b32 s42, 1, s40\ns_cselect_b32 s41, -1, 1\n" \
  "s_cselect_b32 s3, 7, 13\ns_cselect_b64 s[4:5], -1, 0\ns_cselect_b32 s6, 3, 7\ns_lshl_b32 s3, 1, s3\ns_add_u32 s42, s42, s29\ns_add_u32 s43, s42, s41\n" \
  "s_add_u32 s44, s42, 2\ns_and_b32 s42, s42, 7\ns_and_b32 s43, s43, 7\ns_and_b32 s44, s44, 7\ns_mul_i32 s6, s6, 0x01010101\ns_mov_b32 s7, s6\ns_lshl_b32 s40, s42, 10\n" \
  "v_add_u32 v134, s40, v2\ns_lshl_b32 s40, s43, 10\nv_add_u32 v135, s40, v2\ns_lshl_b32 s40, s44, 10\nv_add_u32 v136, s40, v2\nv_lshrrev_b32 v7, 3, v1\n" \
  "v_and_b32 v8, 7, v1\ns_lshl_b32 s40, s43, 3\nv_add_u32 v9, s40, v7\nv_bfe_u32 v9, v9, 1, 3\nv_xor_b32 v9, v9, v8\nv_lshlrev_b32 v9, 4, v9\nv_add_u32 v9, 156672, v9\n" \
  "v_cndmask_b32 v136, v136, v9, s[4:5]\nv_mov_b32 v6, s44\nv_mov_b32 v9, s43\nv_mov_b32 v8, s42\ns_mov_b32 s46, 0x01010101\ns_mov_b32 s47, s46\ns_mov_b32 s44, 0x02020202\n" \
  "s_mov_b32 s45, s44\nv_cndmask_b32 v6, v6, v9, s[44:45]\nv_cndmask_b32 v6, v6, v8, s[46:47]\nv_lshl_add_u32 v6, v6, 3, v7\nv_lshlrev_b32 v6, 2, v6\n" \
  "v_add_u32 v137, 155904, v6\nv_mov_b64 v[164:165], 0\nv_mov_b64 v[166:167], 0\nv_mov_b64 v[168:169], 0\nv_mov_b64 v[170:171], 0\nv_mov_b64 v[172:173], 0\n" \
  "v_mov_b32 v6, 155648\ns_barrier\nds_read_b64 v[4:5], v6\nds_read_b128 v[140:143], v134 offset:0\nds_read_b128 v[144:147], v135 offset:0\nds_read_b128 v[148:151], v136\n" \
  "v_add_u32 v136, s3, v136\ns_waitcnt lgkmcnt(0)\nv_cndmask_b32 v176, v148, v144, s[4:5]\nv_cndmask_b32 v177, v149, v145, s[4:5]\nv_cndmask_b32 v178, v150, v146, s[4:5]\n" \
  "v_cndmask_b32 v179, v151, v147, s[4:5]\nv_dot2c_f32_bf16 v164, v140, v48\nv_dot2c_f32_bf16 v166, v140, v140\nv_dot2c_f32_bf16 v168, v144, v48\n" \
  "v_dot2c_f32_bf16 v170, v144, v144\nv_dot2c_f32_bf16 v172, v148, v48\nv_dot2c_f32_bf16 v173, v148, v176\nv_dot2c_f32_bf16 v164, v141, v48\n" \
  "v_dot2c_f32_bf16 v166, v141, v141\nv_dot2c_f32_bf16 v168, v145, v48\nv_dot2c_f32_bf16 v170, v145, v145\nv_dot2c_f32_bf16 v172, v149, v48\n" \
  "v_dot2c_f32_bf16 v173, v149, v177\nv_dot2c_f32_bf16 v164, v142, v48\nv_dot2c_f32_bf16 v166, v142, v142\nv_dot2c_f32_bf16 v168, v146, v48\n" \
  "v_dot2c_f32_bf16 v170, v146, v146\nv_dot2c_f32_bf16 v172, v150, v48\nv_dot2c_f32_bf16 v173, v150, v178\nv_dot2c_f32_bf16 v164, v143, v48\n" \
  "v_dot2c_f32_bf16 v166, v143, v143\nv_dot2c_f32_bf16 v168, v147, v48\nv_dot2c_f32_bf16 v170, v147, v147\nv_dot2c_f32_bf16 v172, v151, v48\n" \
  "v_dot2c_f32_bf16 v173, v151, v179\ns_barrier\nds_read_b128 v[140:143], v134 offset:8192\nds_read_b128 v[144:147], v135 offset:8192\nds_read_b128 v[148:151], v136\n" \
  "v_add_u32 v136, s3, v136\nds_read_b128 v[152:155], v134 offset:16384\nds_read_b128 v[156:159], v135 offset:16384\nds_read_b128 v[160:163], v136\n" \
  "v_add_u32 v136, s3, v136\nv_readfirstlane_b32 s48, v4\nv_readfirstlane_b32 s49, v5\ns_add_u32 s48, s48, 1\ns_addc_u32 s49, s49, 0\ns_mul_i32 s35, s48, 0x9e3779b1\n" \
  "s_mul_i32 s36, s49, 0x85ebca77\ns_add_u32 s35, s35, s36\ns_lshl_b32 s36, s35, 1\ns_waitcnt lgkmcnt(3)\nv_cndmask_b32 v176, v148, v144, s[4:5]\n" \
  "v_cndmask_b32 v177, v149, v145, s[4:5]\nv_cndmask_b32 v178, v150, v146, s[4:5]\nv_cndmask_b32 v179, v151, v147, s[4:5]\nv_dot2c_f32_bf16 v164, v140, v48\n" \
  "v_dot2c_f32_bf16 v166, v140, v140\nv_dot2c_f32_bf16 v168, v144, v48\nv_dot2c_f32_bf16 v170, v144, v144\nv_dot2c_f32_bf16 v172, v148, v48\n" \
  "v_dot2c_f32_bf16 v173, v148, v176\nv_dot2c_f32_bf16 v164, v141, v48\nv_dot2c_f32_bf16 v166, v141, v141\nv_dot2c_f32_bf16 v168, v145, v48\n" \
  "v_dot2c_f32_bf16 v170, v145, v145\nv_dot2c_f32_bf16 v172, v149, v48\nv_dot2c_f32_bf16 v173, v149, v177\nv_dot2c_f32_bf16 v164, v142, v48\n" \
  "v_dot2c_f32_bf16 v166, v142, v142\nv_dot2c_f32_bf16 v168, v146, v48\nv_dot2c_f32_bf16 v170, v146, v146\nv_dot2c_f32_bf16 v172, v150, v48\n" \
  "v_dot2c_f32_bf16 v173, v150, v178\nv_dot2c_f32_bf16 v164, v143, v48\nv_dot2c_f32_bf16 v166, v143, v143\nv_dot2c_f32_bf16 v168, v147, v48\n" \
  "v_dot2c_f32_bf16 v170, v147, v147\nv_dot2c_f32_bf16 v172, v151, v48\nv_dot2c_f32_bf16 v173, v151, v179\ns_waitcnt lgkmcnt(0)\nv_cndmask_b32 v176, v160, v156, s[4:5]\n" \
  "v_cndmask_b32 v177, v161, v157, s[4:5]\nv_cndmask_b32 v178, v162, v158, s[4:5]\nv_cndmask_b32 v179, v163, v159, s[4:5]\nv_dot2c_f32_bf16 v164, v152, v48\n" \
  "v_dot2c_f32_bf16 v166, v152, v152\nv_dot2c_f32_bf16 v168, v156, v48\nv_dot2c_f32_bf16 v170, v156, v156\nv_dot2c_f32_bf16 v172, v160, v48\n" \
  "v_dot2c_f32_bf16 v173, v160, v176\nv_dot2c_f32_bf16 v164, v153, v48\nv_dot2c_f32_bf16 v166, v153, v153\nv_dot2c_f32_bf16 v168, v157, v48\n" \
  "v_dot2c_f32_bf16 v170, v157, v157\nv_dot2c_f32_bf16 v172, v161, v48\nv_dot2c_f32_bf16 v173, v161, v177\nv_dot2c_f32_bf16 v164, v154, v48\n" \
  "v_dot2c_f32_bf16 v166, v154, v154\nv_dot2c_f32_bf16 v168, v158, v48\nv_dot2c_f32_bf16 v170, v158, v158\nv_dot2c_f32_bf16 v172, v162, v48\n" \
  "v_dot2c_f32_bf16 v173, v162, v178\nv_dot2c_f32_bf16 v164, v155, v48\nv_dot2c_f32_bf16 v166, v155, v155\nv_dot2c_f32_bf16 v168, v159, v48\n" \
  "v_dot2c_f32_bf16 v170, v159, v159\nv_dot2c_f32_bf16 v172, v163, v48\nv_dot2c_f32_bf16 v173, v163, v179\ns_barrier\nds_read_b128 v[140:143], v134 offset:24576\n" \
  "ds_read_b128 v[144:147], v135 offset:24576\nds_read_b128 v[148:151], v136\nv_add_u32 v136, s3, v136\nds_read_b128 v[152:155], v134 offset:32768\n" \
  "ds_read_b128 v[156:159], v135 offset:32768\nds_read_b128 v[160:163], v136\nv_add_u32 v136, s3, v136\ns_waitcnt lgkmcnt(5)\nv_lshlrev_b32 v180, 16, v140\n" \
  "v_and_b32 v181, 0xffff0000, v140\nv_add_f32 v164, v180, v164\nv_add_f32 v165, v181, v165\nv_fmac_f32 v166, v180, v180\nv_fmac_f32 v167, v181, v181\n" \
  "v_lshlrev_b32 v182, 16, v141\nv_and_b32 v183, 0xffff0000, v141\nv_add_f32 v164, v182, v164\nv_add_f32 v165, v183, v165\nv_fmac_f32 v166, v182, v182\n" \
  "v_fmac_f32 v167, v183, v183\nv_lshlrev_b32 v180, 16, v142\nv_and_b32 v181, 0xffff0000, v142\nv_add_f32 v164, v180, v164\nv_add_f32 v165, v181, v165\n" \
  "v_fmac_f32 v166, v180, v180\nv_fmac_f32 v167, v181, v181\nv_lshlrev_b32 v182, 16, v143\nv_and_b32 v183, 0xffff0000, v143\nv_add_f32 v164, v182, v164\n" \
  "v_add_f32 v165, v183, v165\nv_fmac_f32 v166, v182, v182\nv_fmac_f32 v167, v183, v183\ns_waitcnt lgkmcnt(3)\nv_cndmask_b32 v176, v148, v144, s[4:5]\n" \
  "v_cndmask_b32 v177, v149, v145, s[4:5]\nv_cndmask_b32 v178, v150, v146, s[4:5]\nv_cndmask_b32 v179, v151, v147, s[4:5]\nv_dot2c_f32_bf16 v168, v144, v48\n" \
  "v_dot2c_f32_bf16 v170, v144, v144\nv_dot2c_f32_bf16 v172, v148, v48\nv_dot2c_f32_bf16 v173, v148, v176\nv_dot2c_f32_bf16 v168, v145, v48\n" \
  "v_dot2c_f32_bf16 v170, v145, v145\nv_dot2c_f32_bf16 v172, v149, v48\nv_dot2c_f32_bf16 v173, v149, v177\nv_dot2c_f32_bf16 v168, v146, v48\n" \
  "v_dot2c_f32_bf16 v170, v146, v146\nv_dot2c_f32_bf16 v172, v150, v48\nv_dot2c_f32_bf16 v173, v150, v178\nv_dot2c_f32_bf16 v168, v147, v48\n" \
  "v_dot2c_f32_bf16 v170, v147, v147\nv_dot2c_f32_bf16 v172, v151, v48\nv_dot2c_f32_bf16 v173, v151, v179\ns_waitcnt lgkmcnt(2)\nv_lshlrev_b32 v180, 16, v152\n" \
  "v_and_b32 v181, 0xffff0000, v152\nv_add_f32 v164, v180, v164\nv_add_f32 v165, v181, v165\nv_fmac_f32 v166, v180, v180\nv_fmac_f32 v167, v181, v181\n" \
  "v_lshlrev_b32 v182, 16, v153\nv_and_b32 v183, 0xffff0000, v153\nv_add_f32 v164, v182, v164\nv_add_f32 v165, v183, v165\nv_fmac_f32 v166, v182, v182\n" \
  "v_fmac_f32 v167, v183, v183\nv_lshlrev_b32 v180, 16, v154\nv_and_b32 v181, 0xffff0000, v154\nv_add_f32 v164, v180, v164\nv_add_f32 v165, v181, v165\n" \
  "v_fmac_f32 v166, v180, v180\nv_fmac_f32 v167, v181, v181\nv_lshlrev_b32 v182, 16, v155\nv_and_b32 v183, 0xffff0000, v155\nv_add_f32 v164, v182, v164\n" \
  "v_add_f32 v165, v183, v165\nv_fmac_f32 v166, v182, v182\nv_fmac_f32 v167, v183, v183\ns_waitcnt lgkmcnt(0)\nv_cndmask_b32 v176, v160, v156, s[4:5]\n" \
  "v_cndmask_b32 v177, v161, v157, s[4:5]\nv_cndmask_b32 v178, v162, v158, s[4:5]\nv_cndmask_b32 v179, v163, v159, s[4:5]\nv_dot2c_f32_bf16 v168, v156, v48\n" \
  "v_dot2c_f32_bf16 v170, v156, v156\nv_dot2c_f32_bf16 v172, v160, v48\nv_dot2c_f32_bf16 v173, v160, v176\nv_dot2c_f32_bf16 v168, v157, v48\n" \
  "v_dot2c_f32_bf16 v170, v157, v157\nv_dot2c_f32_bf16 v172, v161, v48\nv_dot2c_f32_bf16 v173, v161, v177\nv_dot2c_f32_bf16 v168, v158, v48\n" \
  "v_dot2c_f32_bf16 v170, v158, v158\nv_dot2c_f32_bf16 v172, v162, v48\nv_dot2c_f32_bf16 v173, v162, v178\nv_dot2c_f32_bf16 v168, v159, v48\n" \
  "v_dot2c_f32_bf16 v170, v159, v159\nv_dot2c_f32_bf16 v172, v163, v48\nv_dot2c_f32_bf16 v173, v163, v179\ns_barrier\nds_read_b128 v[140:143], v134 offset:40960\n" \
  "ds_read_b128 v[144:147], v135 offset:40960\nds_read_b128 v[148:151], v136\nv_add_u32 v136, s3, v136\ns_waitcnt lgkmcnt(2)\nv_lshlrev_b32 v180, 16, v140\n" \
  "v_and_b32 v181, 0xffff0000, v140\nv_add_f32 v164, v180, v164\nv_add_f32 v165, v181, v165\nv_fmac_f32 v166, v180, v180\nv_fmac_f32 v167, v181, v181\n" \
  "v_lshlrev_b32 v182, 16, v141\nv_and_b32 v183, 0xffff0000, v141\nv_add_f32 v164, v182, v164\nv_add_f32 v165, v183, v165\nv_fmac_f32 v166, v182, v182\n" \
  "v_fmac_f32 v167, v183, v183\nv_lshlrev_b32 v180, 16, v142\nv_and_b32 v181, 0xffff0000, v142\nv_add_f32 v164, v180, v164\nv_add_f32 v165, v181, v165\n" \
  "v_fmac_f32 v166, v180, v180\nv_fmac_f32 v167, v181, v181\nv_lshlrev_b32 v182, 16, v143\nv_and_b32 v183, 0xffff0000, v143\nv_add_f32 v164, v182, v164\n" \
  "v_add_f32 v165, v183, v165\nv_fmac_f32 v166, v182, v182\nv_fmac_f32 v167, v183, v183\ns_waitcnt lgkmcnt(0)\nv_cndmask_b32 v176, v148, v144, s[4:5]\n" \
  "v_cndmask_b32 v177, v149, v145, s[4:5]\nv_cndmask_b32 v178, v150, v146, s[4:5]\nv_cndmask_b32 v179, v151, v147, s[4:5]\nv_dot2c_f32_bf16 v168, v144, v48\n" \
  "v_dot2c_f32_bf16 v170, v144, v144\nv_dot2c_f32_bf16 v172, v148, v48\nv_dot2c_f32_bf16 v173, v148, v176\nv_dot2c_f32_bf16 v168, v145, v48\n" \
  "v_dot2c_f32_bf16 v170, v145, v145\nv_dot2c_f32_bf16 v172, v149, v48\nv_dot2c_f32_bf16 v173, v149, v177\nv_dot2c_f32_bf16 v168, v146, v48\n" \
  "v_dot2c_f32_bf16 v170, v146, v146\nv_dot2c_f32_bf16 v172, v150, v48\nv_dot2c_f32_bf16 v173, v150, v178\nv_dot2c_f32_bf16 v168, v147, v48\n" \
  "v_dot2c_f32_bf16 v170, v147, v147\nv_dot2c_f32_bf16 v172, v151, v48\nv_dot2c_f32_bf16 v173, v151, v179\ns_barrier\nds_read_b128 v[140:143], v134 offset:49152\n" \
  "ds_read_b128 v[144:147], v135 offset:49152\nds_read_b128 v[148:151], v136\nv_add_u32 v136, s3, v136\ns_waitcnt lgkmcnt(2)\nv_lshlrev_b32 v180, 16, v140\n" \
  "v_and_b32 v181, 0xffff0000, v140\nv_add_f32 v164, v180, v164\nv_add_f32 v165, v181, v165\nv_fmac_f32 v166, v180, v180\nv_fmac_f32 v167, v181, v181\n" \
  "v_lshlrev_b32 v182, 16, v141\nv_and_b32 v183, 0xffff0000, v141\nv_add_f32 v164, v182, v164\nv_add_f32 v165, v183, v165\nv_fmac_f32 v166, v182, v182\n" \
  "v_fmac_f32 v167, v183, v183\nv_lshlrev_b32 v180, 16, v142\nv_and_b32 v181, 0xffff0000, v142\nv_add_f32 v164, v180, v164\nv_add_f32 v165, v181, v165\n" \
  "v_fmac_f32 v166, v180, v180\nv_fmac_f32 v167, v181, v181\nv_lshlrev_b32 v182, 16, v143\nv_and_b32 v183, 0xffff0000, v143\nv_add_f32 v164, v182, v164\n" \
  "v_add_f32 v165, v183, v165\nv_fmac_f32 v166, v182, v182\nv_fmac_f32 v167, v183, v183\ns_waitcnt lgkmcnt(0)\nv_cndmask_b32 v176, v148, v144, s[4:5]\n" \
  "v_cndmask_b32 v177, v149, v145, s[4:5]\nv_cndmask_b32 v178, v150, v146, s[4:5]\nv_cndmask_b32 v179, v151, v147, s[4:5]\nv_dot2c_f32_bf16 v168, v144, v48\n" \
  "v_dot2c_f32_bf16 v170, v144, v144\nv_dot2c_f32_bf16 v172, v148, v48\nv_dot2c_f32_bf16 v173, v148, v176\nv_dot2c_f32_bf16 v168, v145, v48\n" \
  "v_dot2c_f32_bf16 v170, v145, v145\nv_dot2c_f32_bf16 v172, v149, v48\nv_dot2c_f32_bf16 v173, v149, v177\nv_dot2c_f32_bf16 v168, v146, v48\n" \
  "v_dot2c_f32_bf16 v170, v146, v146\nv_dot2c_f32_bf16 v172, v150, v48\nv_dot2c_f32_bf16 v173, v150, v178\nv_dot2c_f32_bf16 v168, v147, v48\n" \
  "v_dot2c_f32_bf16 v170, v147, v147\nv_dot2c_f32_bf16 v172, v151, v48\nv_dot2c_f32_bf16 v173, v151, v179\ns_barrier\nds_read_b128 v[140:143], v134 offset:57344\n" \
  "ds_read_b128 v[144:147], v135 offset:57344\nds_read_b128 v[148:151], v136\nv_add_u32 v136, s3, v136\ns_waitcnt lgkmcnt(2)\nv_lshlrev_b32 v180, 16, v140\n" \
  "v_and_b32 v181, 0xffff0000, v140\nv_add_f32 v164, v180, v164\nv_add_f32 v165, v181, v165\nv_fmac_f32 v166, v180, v180\nv_fmac_f32 v167, v181, v181\n" \
  "v_lshlrev_b32 v182, 16, v141\nv_and_b32 v183, 0xffff0000, v141\nv_add_f32 v164, v182, v164\nv_add_f32 v165, v183, v165\nv_fmac_f32 v166, v182, v182\n" \
  "v_fmac_f32 v167, v183, v183\nv_lshlrev_b32 v180, 16, v142\nv_and_b32 v181, 0xffff0000, v142\nv_add_f32 v164, v180, v164\nv_add_f32 v165, v181, v165\n" \
  "v_fmac_f32 v166, v180, v180\nv_fmac_f32 v167, v181, v181\nv_lshlrev_b32 v182, 16, v143\nv_and_b32 v183, 0xffff0000, v143\nv_add_f32 v164, v182, v164\n" \
  "v_add_f32 v165, v183, v165\nv_fmac_f32 v166, v182, v182\nv_fmac_f32 v167, v183, v183\ns_waitcnt lgkmcnt(0)\nv_cndmask_b32 v176, v148, v144, s[4:5]\n" \
  "v_cndmask_b32 v177, v149, v145, s[4:5]\nv_cndmask_b32 v178, v150, v146, s[4:5]\nv_cndmask_b32 v179, v151, v147, s[4:5]\nv_dot2c_f32_bf16 v168, v144, v48\n" \
  "v_dot2c_f32_bf16 v170, v144, v144\nv_dot2c_f32_bf16 v172, v148, v48\nv_dot2c_f32_bf16 v173, v148, v176\nv_dot2c_f32_bf16 v168, v145, v48\n" \
  "v_dot2c_f32_bf16 v170, v145, v145\nv_dot2c_f32_bf16 v172, v149, v48\nv_dot2c_f32_bf16 v173, v149, v177\nv_dot2c_f32_bf16 v168, v146, v48\n" \
  "v_dot2c_f32_bf16 v170, v146, v146\nv_dot2c_f32_bf16 v172, v150, v48\nv_dot2c_f32_bf16 v173, v150, v178\nv_dot2c_f32_bf16 v168, v147, v48\n" \
  "v_dot2c_f32_bf16 v170, v147, v147\nv_dot2c_f32_bf16 v172, v151, v48\nv_dot2c_f32_bf16 v173, v151, v179\ns_barrier\ns_nop 0\nv_add_f32 v164, v164, v165\n" \
  "v_add_f32 v166, v166, v167\nv_add_f32 v168, v168, v169\nv_add_f32 v170, v170, v171\nv_add_f32_dpp v172, v172, v172 quad_perm:[1,0,3,2] row_mask:0xf bank_mask:0xf\n" \
  "v_add_f32_dpp v173, v173, v173 quad_perm:[1,0,3,2] row_mask:0xf bank_mask:0xf\nv_add_f32_dpp v164, v164, v164 quad_perm:[1,0,3,2] row_mask:0xf bank_mask:0xf\n" \
  "v_add_f32_dpp v166, v166, v166 quad_perm:[1,0,3,2] row_mask:0xf bank_mask:0xf\nv_add_f32_dpp v168, v168, v168 quad_perm:[1,0,3,2] row_mask:0xf bank_mask:0xf\n" \
  "v_add_f32_dpp v170, v170, v170 quad_perm:[1,0,3,2] row_mask:0xf bank_mask:0xf\nv_add_f32_dpp v172, v172, v172 quad_perm:[2,3,0,1] row_mask:0xf bank_mask:0xf\n" \
  "v_add_f32_dpp v173, v173, v173 quad_perm:[2,3,0,1] row_mask:0xf bank_mask:0xf\nv_add_f32_dpp v164, v164, v164 quad_perm:[2,3,0,1] row_mask:0xf bank_mask:0xf\n" \
  "v_add_f32_dpp v166, v166, v166 quad_perm:[2,3,0,1] row_mask:0xf bank_mask:0xf\nv_add_f32_dpp v168, v168, v168 quad_perm:[2,3,0,1] row_mask:0xf bank_mask:0xf\n" \
  "v_add_f32_dpp v170, v170, v170 quad_perm:[2,3,0,1] row_mask:0xf bank_mask:0xf\nv_add_f32_dpp v172, v172, v172 row_half_mirror row_mask:0xf bank_mask:0xf\n" \
  "v_add_f32_dpp v173, v173, v173 row_half_mirror row_mask:0xf bank_mask:0xf\nv_add_f32_dpp v164, v164, v164 row_half_mirror row_mask:0xf bank_mask:0xf\n" \
  "v_add_f32_dpp v166, v166, v166 row_half_mirror row_mask:0xf bank_mask:0xf\nv_add_f32_dpp v168, v168, v168 row_half_mirror row_mask:0xf bank_mask:0xf\n" \
  "v_add_f32_dpp v170, v170, v170 row_half_mirror row_mask:0xf bank_mask:0xf\nv_cndmask_b32 v6, v172, v168, s[44:45]\nv_cndmask_b32 v7, v173, v170, s[44:45]\n" \
  "v_cndmask_b32 v6, v6, v164, s[46:47]\nv_cndmask_b32 v7, v7, v166, s[46:47]\nv_mul_f32 v6, 0x3b000000, v6\nv_mul_f32 v7, 0x3b000000, v7\nv_fma_f32 v7, -v6, v6, v7\n" \
  "v_max_f32 v7, 0, v7\nv_cvt_pk_bf16_f32 v7, v7, v7\nv_cvt_pk_bf16_f32 v6, -v6, -v6\nv_and_b32 v7, 0xffff0000, v7\nv_add_f32 v7, 0x3727c5ac, v7\n" \
  "v_cvt_pk_bf16_f32 v7, v7, v7\nv_and_b32 v7, 0xffff0000, v7\nv_sqrt_f32 v7, v7\ns_nop 0\nv_cvt_pk_bf16_f32 v7, v7, v7\nv_and_b32 v7, 0xffff0000, v7\nv_rcp_f32 v8, v7\n" \
  "s_mov_b64 exec, s[6:7]\nds_write2_b32 v137, v6, v8 offset1:64\ns_mov_b64 exec, -1\ns_barrier\ns_cmp_lg_u32 s27, 4\ns_cbranch_scc1 .Lfz_h4\nv_and_b32 v9, 0xffff0000, v6\n" \
  "v_fma_f32 v9, v9, v172, v173\nv_mul_f32 v9, v9, v8\nv_cvt_pk_bf16_f32 v9, v9, v9\nv_ashrrev_i32 v144, 31, v9\nv_or_b32 v144, 0x80000000, v144\nv_xor_b32 v9, v9, v144\n" \
  "v_and_b32 v9, 0xffff0000, v9\ns_lshl_b32 s40, s28, 3\ns_add_u32 s40, s40, s29\ns_lshl_b32 s40, s40, 3\ns_xor_b32 s41, s40, 2047\nv_lshrrev_b32 v144, 3, v1\n" \
  "v_xor_b32 v144, s41, v144\nv_or_b32 v140, v9, v144\nv_cndmask_b32 v140, 0, v140, s[44:45]\nv_mov_b32 v141, s48\nv_and_b32 v142, 0xffff, v6\n" \
  "v_max_u32_dpp v146, v140, v140 row_ror:8 row_mask:0xf bank_mask:0xf\nv_or_b32 v142, v142, v7\nv_mov_b32 v147, v146\nv_mov_b32 v143, s48\ns_add_u32 s40, s40, 4096\n" \
  "v_permlane16_swap_b32 v146, v147\nv_mov_b32 v9, s40\nv_max_u32 v146, v146, v147\nv_mov_b32 v147, v146\ns_nop 1\nv_permlane32_swap_b32 v146, v147\ns_nop 0\n" \
  "v_max_u32 v146, v146, v147\nv_cmp_eq_u32 vcc, v146, v140\ns_waitcnt lgkmcnt(0)\ns_barrier\ns_and_b64 exec, vcc, s[44:45]\n" \
  "global_store_dwordx2 v9, v[140:141], s[24:25] sc1\nglobal_store_dwordx2 v9, v[142:143], s[24:25] offset:2048 sc1\ns_mov_b64 exec, -1\ns_branch .Lfz_q\n.Lfz_h4:\n" \
  "v_lshlrev_b32 v6, 2, v1\nv_add_u32 v6, 155904, v6\nv_mov_b32 v233, s48\nv_mov_b32 v235, s48\nv_add_u32 v7, 26112, v2\ns_waitcnt lgkmcnt(0)\ns_barrier\n" \
  "s_cmp_lg_u32 s27, 5\ns_cbranch_scc1 .Lfz_q\nds_read_b32 v232, v6\nds_read_b32 v234, v6 offset:256\ns_waitcnt lgkmcnt(0)\n" \
  "global_store_dwordx4 v7, v[232:235], s[38:39] sc1\n.Lfz_q:\nv_xor_b32 v208, 4096, v252\nv_xor_b32 v209, 4096, v253\nds_read_b32 v240, v254\n" \
  "ds_read_b32 v8, v254 offset:256\nds_read_b128 v[128:131], v248 offset:0\nds_read_b64_tr_b16 v[132:133], v253 offset:24576\n" \
  "ds_read_b64_tr_b16 v[134:135], v253 offset:25088\nds_read_b128 v[136:139], v249 offset:0\nds_read_b64_tr_b16 v[140:141], v253 offset:26624\n" \
  "ds_read_b64_tr_b16 v[142:143], v253 offset:27136\nds_read_b128 v[144:147], v250 offset:0\nds_read_b64_tr_b16 v[148:149], v209 offset:24576\n" \
  "ds_read_b64_tr_b16 v[150:151], v209 offset:25088\nds_read_b128 v[152:155], v251 offset:0\nds_read_b64_tr_b16 v[156:157], v209 offset:26624\n" \
  "ds_read_b64_tr_b16 v[158:159], v209 offset:27136\ns_and_b32 s40, s27, 3\ns_lshl_b32 s41, s40, 7\ns_add_u32 s41, s41, 160768\nv_lshl_add_u32 v254, v1, 2, s41\n" \
  "v_xor_b32 v7, 0x100, v254\ns_lshl_b32 s40, s40, 11\ns_add_u32 s40, s40, 65536\nv_add_u32 v255, s40, v2\nv_mov_b64 v[242:243], 0\nv_mov_b32 v241, 0\nv_mov_b32 v245, 0\n" \
  "v_mov_b64 v[246:247], 0\nv_mov_b32 v6, 0\ns_waitcnt lgkmcnt(9)\nv_mfma_f32_32x32x16_bf16 v[192:207], v[132:135], v[128:131], 0\n" \
  "ds_read_b128 v[160:163], v248 offset:8192\nds_read_b64_tr_b16 v[164:165], v253 offset:32768\nds_read_b64_tr_b16 v[166:167], v253 offset:33280\ns_waitcnt lgkmcnt(9)\n" \
  "v_mfma_f32_32x32x16_bf16 v[192:207], v[140:143], v[136:139], v[192:207]\nv_mul_f32 v8, 0x3eb8aa3b, v8\nds_read_b128 v[168:171], v249 offset:8192\n" \
  "ds_read_b64_tr_b16 v[172:173], v253 offset:34816\nds_read_b64_tr_b16 v[174:175], v253 offset:35328\nv_mfma_f32_16x16x32_bf16 v[224:227], v[228:231], v[132:135], 0\n" \
  "v_mfma_f32_16x16x32_bf16 v[224:227], v[228:231], v[140:143], v[224:227]\ns_waitcnt lgkmcnt(9)\nv_mfma_f32_32x32x16_bf16 v[192:207], v[148:151], v[144:147], v[192:207]\n" \
  "ds_read_b128 v[176:179], v250 offset:8192\nds_read_b64_tr_b16 v[180:181], v209 offset:32768\nds_read_b64_tr_b16 v[182:183], v209 offset:33280\ns_waitcnt lgkmcnt(9)\n" \
  "v_mfma_f32_32x32x16_bf16 v[192:207], v[156:159], v[152:155], v[192:207]\nds_read_b128 v[184:187], v251 offset:8192\nds_read_b64_tr_b16 v[188:189], v209 offset:34816\n" \
  "ds_read_b64_tr_b16 v[190:191], v209 offset:35328\ns_waitcnt lgkmcnt(9)\nv_mfma_f32_32x32x16_bf16 v[192:207], v[164:167], v[160:163], v[192:207]\n" \
  "ds_read_b128 v[128:131], v248 offset:16384\nds_read_b64_tr_b16 v[132:133], v253 offset:16384\nds_read_b64_tr_b16 v[134:135], v253 offset:16896\ns_waitcnt lgkmcnt(9)\n" \
  "v_mfma_f32_32x32x16_bf16 v[192:207], v[172:175], v[168:171], v[192:207]\nds_read_b128 v[136:139], v249 offset:16384\nds_read_b64_tr_b16 v[140:141], v253 offset:18432\n" \
  "ds_read_b64_tr_b16 v[142:143], v253 offset:18944\nv_mfma_f32_16x16x32_bf16 v[224:227], v[228:231], v[164:167], v[224:227]\n" \
  "v_mfma_f32_16x16x32_bf16 v[224:227], v[228:231], v[172:175], v[224:227]\ns_waitcnt lgkmcnt(9)\nv_mfma_f32_32x32x16_bf16 v[192:207], v[180:183], v[176:179], v[192:207]\n" \
  "ds_read_b128 v[144:147], v250 offset:16384\nds_read_b64_tr_b16 v[148:149], v209 offset:16384\nds_read_b64_tr_b16 v[150:151], v209 offset:16896\ns_waitcnt lgkmcnt(9)\n" \
  "v_mfma_f32_32x32x16_bf16 v[192:207], v[188:191], v[184:187], v[192:207]\nds_read_b128 v[152:155], v251 offset:16384\nds_read_b64_tr_b16 v[156:157], v209 offset:18432\n" \
  "ds_read_b64_tr_b16 v[158:159], v209 offset:18944\ns_waitcnt lgkmcnt(9)\nv_mfma_f32_32x32x16_bf16 v[192:207], v[132:135], v[128:131], v[192:207]\n" \
  "ds_read_b128 v[160:163], v248 offset:24576\nds_read_b64_tr_b16 v[164:165], v252 offset:0\nds_read_b64_tr_b16 v[166:167], v252 offset:512\ns_waitcnt lgkmcnt(9)\n" \
  "v_mfma_f32_32x32x16_bf16 v[192:207], v[140:143], v[136:139], v[192:207]\nds_read_b128 v[168:171], v249 offset:24576\nds_read_b64_tr_b16 v[172:173], v252 offset:2048\n" \
  "ds_read_b64_tr_b16 v[174:175], v252 offset:2560\nv_mfma_f32_16x16x32_bf16 v[224:227], v[228:231], v[132:135], v[224:227]\n" \
  "v_mfma_f32_16x16x32_bf16 v[224:227], v[228:231], v[140:143], v[224:227]\ns_waitcnt lgkmcnt(9)\nv_mfma_f32_32x32x16_bf16 v[192:207], v[148:151], v[144:147], v[192:207]\n" \
  "ds_read_b128 v[176:179], v250 offset:24576\nds_read_b64_tr_b16 v[180:181], v208 offset:0\nds_read_b64_tr_b16 v[182:183], v208 offset:512\ns_waitcnt lgkmcnt(9)\n" \
  "v_mfma_f32_32x32x16_bf16 v[192:207], v[156:159], v[152:155], v[192:207]\nds_read_b128 v[184:187], v251 offset:24576\nds_read_b64_tr_b16 v[188:189], v208 offset:2048\n" \
  "ds_read_b64_tr_b16 v[190:191], v208 offset:2560\ns_waitcnt lgkmcnt(9)\nv_mfma_f32_32x32x16_bf16 v[192:207], v[164:167], v[160:163], v[192:207]\ns_cmp_lg_u32 s27, 7\n" \
  "s_cbranch_scc1 .Lfz_q9\ns_waitcnt vmcnt(0)\n.Lfz_q9:\ns_barrier\nds_read_b128 v[128:131], v248 offset:32768\nds_read_b64_tr_b16 v[132:133], v252 offset:8192\n" \
  "ds_read_b64_tr_b16 v[134:135], v252 offset:8704\ns_waitcnt lgkmcnt(9)\nv_mfma_f32_32x32x16_bf16 v[192:207], v[172:175], v[168:171], v[192:207]\n" \
  "ds_read_b128 v[136:139], v249 offset:32768\nds_read_b64_tr_b16 v[140:141], v252 offset:10240\nds_read_b64_tr_b16 v[142:143], v252 offset:10752\n" \
  "v_mfma_f32_16x16x32_bf16 v[224:227], v[228:231], v[164:167], v[224:227]\nv_mfma_f32_16x16x32_bf16 v[224:227], v[228:231], v[172:175], v[224:227]\ns_waitcnt lgkmcnt(9)\n" \
  "v_mfma_f32_32x32x16_bf16 v[192:207], v[180:183], v[176:179], v[192:207]\nds_read_b128 v[144:147], v250 offset:32768\nds_read_b64_tr_b16 v[148:149], v208 offset:8192\n" \
  "ds_read_b64_tr_b16 v[150:151], v208 offset:8704\ns_waitcnt lgkmcnt(9)\nv_mfma_f32_32x32x16_bf16 v[192:207], v[188:191], v[184:187], v[192:207]\n" \
  "ds_read_b128 v[152:155], v251 offset:32768\nds_read_b64_tr_b16 v[156:157], v208 offset:10240\nds_read_b64_tr_b16 v[158:159], v208 offset:10752\ns_waitcnt lgkmcnt(9)\n" \
  "v_mfma_f32_32x32x16_bf16 v[192:207], v[132:135], v[128:131], v[192:207]\nds_read_b128 v[160:163], v248 offset:40960\nds_read_b64_tr_b16 v[164:165], v252 offset:16384\n" \
  "ds_read_b64_tr_b16 v[166:167], v252 offset:16896\ns_waitcnt lgkmcnt(9)\nv_mfma_f32_32x32x16_bf16 v[192:207], v[140:143], v[136:139], v[192:207]\n" \
  "ds_read_b128 v[168:171], v249 offset:40960\nds_read_b64_tr_b16 v[172:173], v252 offset:18432\nds_read_b64_tr_b16 v[174:175], v252 offset:18944\n" \
  "v_mfma_f32_16x16x32_bf16 v[224:227], v[228:231], v[132:135], v[224:227]\nv_mfma_f32_16x16x32_bf16 v[224:227], v[228:231], v[140:143], v[224:227]\ns_waitcnt lgkmcnt(9)\n" \
  "v_mfma_f32_32x32x16_bf16 v[192:207], v[148:151], v[144:147], v[192:207]\nds_read_b128 v[176:179], v250 offset:40960\nds_read_b64_tr_b16 v[180:181], v208 offset:16384\n" \
  "ds_read_b64_tr_b16 v[182:183], v208 offset:16896\ns_waitcnt lgkmcnt(9)\nv_mfma_f32_32x32x16_bf16 v[192:207], v[156:159], v[152:155], v[192:207]\n" \
  "ds_read_b128 v[184:187], v251 offset:40960\nds_read_b64_tr_b16 v[188:189], v208 offset:18432\nds_read_b64_tr_b16 v[190:191], v208 offset:18944\ns_waitcnt lgkmcnt(9)\n" \
  "v_mfma_f32_32x32x16_bf16 v[192:207], v[164:167], v[160:163], v[192:207]\nds_read_b128 v[128:131], v248 offset:49152\nds_read_b64_tr_b16 v[132:133], v252 offset:24576\n" \
  "ds_read_b64_tr_b16 v[134:135], v252 offset:25088\ns_waitcnt lgkmcnt(9)\nv_mfma_f32_32x32x16_bf16 v[192:207], v[172:175], v[168:171], v[192:207]\n" \
  "ds_read_b128 v[136:139], v249 offset:49152\nds_read_b64_tr_b16 v[140:141], v252 offset:26624\nds_read_b64_tr_b16 v[142:143], v252 offset:27136\n" \
  "v_mfma_f32_16x16x32_bf16 v[224:227], v[228:231], v[164:167], v[224:227]\nv_mfma_f32_16x16x32_bf16 v[224:227], v[228:231], v[172:175], v[224:227]\ns_waitcnt lgkmcnt(9)\n" \
  "v_mfma_f32_32x32x16_bf16 v[192:207], v[180:183], v[176:179], v[192:207]\nds_read_b128 v[144:147], v250 offset:49152\nds_read_b64_tr_b16 v[148:149], v208 offset:24576\n" \
  "ds_read_b64_tr_b16 v[150:151], v208 offset:25088\ns_waitcnt lgkmcnt(9)\nv_mfma_f32_32x32x16_bf16 v[192:207], v[188:191], v[184:187], v[192:207]\n" \
  "ds_read_b128 v[152:155], v251 offset:49152\nds_read_b64_tr_b16 v[156:157], v208 offset:26624\nds_read_b64_tr_b16 v[158:159], v208 offset:27136\ns_waitcnt lgkmcnt(9)\n" \
  "v_mfma_f32_32x32x16_bf16 v[192:207], v[132:135], v[128:131], v[192:207]\nds_read_b128 v[160:163], v248 offset:57344\nds_read_b64_tr_b16 v[164:165], v252 offset:32768\n" \
  "ds_read_b64_tr_b16 v[166:167], v252 offset:33280\ns_waitcnt lgkmcnt(9)\nv_mfma_f32_32x32x16_bf16 v[192:207], v[140:143], v[136:139], v[192:207]\n" \
  "ds_read_b128 v[168:171], v249 offset:57344\nds_read_b64_tr_b16 v[172:173], v252 offset:34816\nds_read_b64_tr_b16 v[174:175], v252 offset:35328\n" \
  "v_mfma_f32_16x16x32_bf16 v[224:227], v[228:231], v[132:135], v[224:227]\nv_mfma_f32_16x16x32_bf16 v[224:227], v[228:231], v[140:143], v[224:227]\ns_waitcnt lgkmcnt(9)\n" \
  "v_mfma_f32_32x32x16_bf16 v[192:207], v[148:151], v[144:147], v[192:207]\nds_read_b128 v[176:179], v250 offset:57344\nds_read_b64_tr_b16 v[180:181], v208 offset:32768\n" \
  "ds_read_b64_tr_b16 v[182:183], v208 offset:33280\ns_waitcnt lgkmcnt(9)\nv_mfma_f32_32x32x16_bf16 v[192:207], v[156:159], v[152:155], v[192:207]\n" \
  "ds_read_b128 v[184:187], v251 offset:57344\nds_read_b64_tr_b16 v[188:189], v208 offset:34816\nds_read_b64_tr_b16 v[190:191], v208 offset:35328\ns_mov_b32 exec_hi, 0\n" \
  "ds_write_b32 v254, v224\ns_mov_b64 exec, -1\ns_waitcnt lgkmcnt(10)\nv_mfma_f32_32x32x16_bf16 v[192:207], v[164:167], v[160:163], v[192:207]\ns_waitcnt lgkmcnt(7)\n" \
  "v_mfma_f32_32x32x16_bf16 v[192:207], v[172:175], v[168:171], v[192:207]\ns_waitcnt lgkmcnt(0)\ns_barrier\ns_mov_b32 exec_hi, 0\nds_read_b32 v6, v7\ns_mov_b64 exec, -1\n" \
  "v_mfma_f32_16x16x32_bf16 v[236:239], v[228:231], v[164:167], 0\nv_mfma_f32_16x16x32_bf16 v[236:239], v[228:231], v[172:175], v[236:239]\ns_waitcnt lgkmcnt(1)\n" \
  "v_mfma_f32_16x16x32_bf16 v[236:239], v[228:231], v[180:183], v[236:239]\nv_mfma_f32_16x16x32_bf16 v[236:239], v[228:231], v[188:191], v[236:239]\ns_waitcnt lgkmcnt(5)\n" \
  "v_mfma_f32_32x32x16_bf16 v[192:207], v[180:183], v[176:179], v[192:207]\ns_waitcnt lgkmcnt(2)\nv_mfma_f32_32x32x16_bf16 v[192:207], v[188:191], v[184:187], v[192:207]\n" \
  "s_waitcnt lgkmcnt(0)\nv_add_f32 v6, v6, v224\ns_nop 1\nv_add_f32 v6, v6, v236\nv_cvt_pk_bf16_f32 v7, v6, v6\nv_and_b32 v7, 0xffff0000, v7\nv_sub_f32 v6, v6, v7\n" \
  "v_cvt_pk_bf16_f32 v244, v7, v6\ns_nop 1\nv_mfma_f32_32x32x16_bf16 v[192:207], v[244:247], v[240:243], v[192:207]\ns_nop 11\n" \
  "v_pk_mul_f32 v[192:193], v[192:193], v[8:9] op_sel_hi:[1,0]\nv_pk_mul_f32 v[194:195], v[194:195], v[8:9] op_sel_hi:[1,0]\n" \
  "v_pk_mul_f32 v[196:197], v[196:197], v[8:9] op_sel_hi:[1,0]\nv_pk_mul_f32 v[198:199], v[198:199], v[8:9] op_sel_hi:[1,0]\nv_cvt_pk_bf16_f32 v232, v192, v193\n" \
  "v_cvt_pk_bf16_f32 v233, v194, v195\nv_cvt_pk_bf16_f32 v234, v196, v197\nv_cvt_pk_bf16_f32 v235, v198, v199\nds_write_b128 v255, v[232:235] offset:0\n" \
  "v_pk_mul_f32 v[200:201], v[200:201], v[8:9] op_sel_hi:[1,0]\nv_pk_mul_f32 v[202:203], v[202:203], v[8:9] op_sel_hi:[1,0]\n" \
  "v_pk_mul_f32 v[204:205], v[204:205], v[8:9] op_sel_hi:[1,0]\nv_pk_mul_f32 v[206:207], v[206:207], v[8:9] op_sel_hi:[1,0]\nv_cvt_pk_bf16_f32 v236, v200, v201\n" \
  "v_cvt_pk_bf16_f32 v237, v202, v203\nv_cvt_pk_bf16_f32 v238, v204, v205\nv_cvt_pk_bf16_f32 v239, v206, v207\nds_write_b128 v255, v[236:239] offset:1024\n.Lfz_attn:\n" \
  "s_lshl_b32 s40, s0, 10\ns_add_u32 s41, s40, 65536\ns_xor_b32 s42, s40, 4096\ns_add_u32 s42, s42, 65536\nv_add_u32 v8, s41, v2\nv_add_u32 v9, s42, v2\n" \
  "s_waitcnt lgkmcnt(0)\ns_barrier\nds_read_b128 v[120:123], v14 offset:40960\nds_read_b128 v[208:211], v8\nds_read_b128 v[124:127], v14 offset:41984\n" \
  "ds_read_b128 v[212:215], v9\ns_cmp_eq_u32 s34, 0\ns_cbranch_scc1 .Lfz_s1\ns_waitcnt lgkmcnt(2)\nv_mfma_f32_32x32x16_bf16 v[160:175], v[120:123], v[208:211], v[16:31]\n" \
  "s_waitcnt lgkmcnt(0)\nv_permlane16_swap_b32 v124, v64\nv_permlane16_swap_b32 v125, v65\nv_permlane16_swap_b32 v126, v66\nv_permlane16_swap_b32 v127, v67\n" \
  "v_mov_b64 v[144:145], 0\nv_mov_b64 v[146:147], 0\nv_mov_b64 v[148:149], 0\nv_mov_b64 v[150:151], 0\nv_mov_b64 v[152:153], 0\ns_nop 1\nv_exp_f32 v160, v160\n" \
  "v_exp_f32 v161, v161\nv_exp_f32 v162, v162\nv_exp_f32 v163, v163\nv_exp_f32 v164, v164\nv_exp_f32 v165, v165\nv_xor_b32 v16, 0xff800000, v16\n" \
  "v_xor_b32 v17, 0xff800000, v17\nv_xor_b32 v18, 0xff800000, v18\nv_xor_b32 v19, 0xff800000, v19\nv_xor_b32 v20, 0xff800000, v20\nv_xor_b32 v21, 0xff800000, v21\n" \
  "v_xor_b32 v22, 0xff800000, v22\nv_xor_b32 v23, 0xff800000, v23\nv_xor_b32 v24, 0xff800000, v24\nv_xor_b32 v25, 0xff800000, v25\nv_xor_b32 v26, 0xff800000, v26\n" \
  "v_xor_b32 v27, 0xff800000, v27\nv_xor_b32 v28, 0xff800000, v28\nv_xor_b32 v29, 0xff800000, v29\nv_xor_b32 v30, 0xff800000, v30\nv_xor_b32 v31, 0xff800000, v31\n" \
  "s_cmp_lg_u32 s11, 0\ns_cbranch_scc1 .Lfz_wdb0\n.Lfz_wdrb0:\nv_exp_f32 v166, v166\nv_exp_f32 v167, v167\nv_exp_f32 v168, v168\nv_exp_f32 v169, v169\n" \
  "v_cvt_pk_bf16_f32 v192, v160, v161\nv_cvt_pk_bf16_f32 v193, v162, v163\nv_cvt_pk_bf16_f32 v194, v164, v165\nv_cvt_pk_bf16_f32 v195, v166, v167\ns_cmp_lt_i32 s32, -3\n" \
  "s_cbranch_scc1 .Lfz_wb0\ns_waitcnt vmcnt(11)\n.Lfz_wb0:\nv_mfma_f32_32x32x16_bf16 v[176:191], v[100:103], v[212:215], 0\nv_add3_u32 v6, v100, v101, v102\n" \
  "v_add3_u32 v7, v103, v116, v117\nv_mfma_f32_32x32x16_bf16 v[128:143], v[124:127], v[192:195], 0\nv_add3_u32 v8, v118, v119, v71\nv_add3_u32 v6, v6, v7, v8\n" \
  "v_exp_f32 v170, v170\nv_exp_f32 v171, v171\nv_add_u32_dpp v6, v6, v6 quad_perm:[1,0,3,2] row_mask:0xf bank_mask:0xf\nv_exp_f32 v172, v172\nv_exp_f32 v173, v173\n" \
  "v_add_u32_dpp v6, v6, v6 quad_perm:[2,3,0,1] row_mask:0xf bank_mask:0xf\nv_exp_f32 v174, v174\nv_exp_f32 v175, v175\nv_cmp_ne_u32 vcc, s36, v6\n" \
  "v_cvt_pk_bf16_f32 v196, v168, v169\nv_cvt_pk_bf16_f32 v197, v170, v171\nv_cvt_pk_bf16_f32 v198, v172, v173\nv_cvt_pk_bf16_f32 v199, v174, v175\ns_cbranch_scc1 .Lfz_xb0\n" \
  "s_cbranch_vccnz .Lfz_rb0\n.Lfz_kb0:\nv_mfma_f32_32x32x16_bf16 v[128:143], v[64:67], v[196:199], v[128:143]\nv_permlane16_swap_b32 v116, v60\n" \
  "v_permlane16_swap_b32 v117, v61\nv_permlane16_swap_b32 v118, v62\nv_permlane16_swap_b32 v119, v63\nv_exp_f32 v176, v176\nv_exp_f32 v177, v177\n" \
  "v_mfma_f32_32x32x16_bf16 v[160:175], v[100:103], v[208:211], 0\nv_exp_f32 v178, v178\nv_exp_f32 v179, v179\nv_exp_f32 v180, v180\nv_exp_f32 v181, v181\n" \
  "v_exp_f32 v182, v182\nv_exp_f32 v183, v183\nv_exp_f32 v184, v184\nv_exp_f32 v185, v185\nv_cvt_pk_bf16_f32 v200, v176, v177\nv_cvt_pk_bf16_f32 v201, v178, v179\n" \
  "v_cvt_pk_bf16_f32 v202, v180, v181\nv_cvt_pk_bf16_f32 v203, v182, v183\nv_exp_f32 v186, v186\nv_exp_f32 v187, v187\n" \
  "v_mfma_f32_32x32x16_bf16 v[144:159], v[116:119], v[200:203], 0\nv_exp_f32 v188, v188\nv_exp_f32 v189, v189\nv_exp_f32 v190, v190\nv_exp_f32 v191, v191\ns_nop 0\n" \
  "v_cvt_pk_bf16_f32 v204, v184, v185\nv_cvt_pk_bf16_f32 v205, v186, v187\nv_cvt_pk_bf16_f32 v206, v188, v189\nv_cvt_pk_bf16_f32 v207, v190, v191\nv_exp_f32 v160, v160\n" \
  "v_exp_f32 v161, v161\nv_mfma_f32_32x32x16_bf16 v[144:159], v[60:63], v[204:207], v[144:159]\nv_exp_f32 v162, v162\nv_exp_f32 v163, v163\nv_exp_f32 v164, v164\n" \
  "v_exp_f32 v165, v165\ns_cmp_lg_u32 s11, 0\ns_cbranch_scc1 .Lfz_wdb2\n.Lfz_wdrb2:\ns_cmp_lt_u32 s18, 43\ns_cbranch_scc0 .Lfz_ad\n" \
  "global_load_dwordx4 v[32:35], v248, s[16:17]\nglobal_load_dwordx4 v[36:39], v249, s[16:17]\nglobal_load_dwordx4 v[40:43], v250, s[16:17]\n" \
  "global_load_dwordx4 v[44:47], v251, s[16:17]\n.Lfz_adr:\nv_exp_f32 v166, v166\nv_exp_f32 v167, v167\nv_exp_f32 v168, v168\nv_exp_f32 v169, v169\n" \
  "v_cvt_pk_bf16_f32 v192, v160, v161\nv_cvt_pk_bf16_f32 v193, v162, v163\nv_cvt_pk_bf16_f32 v194, v164, v165\nv_cvt_pk_bf16_f32 v195, v166, v167\ns_cmp_lt_i32 s32, -2\n" \
  "s_cbranch_scc1 .Lfz_wb2\ns_waitcnt vmcnt(12)\n.Lfz_wb2:\nv_mfma_f32_32x32x16_bf16 v[176:191], v[96:99], v[212:215], 0\nv_add3_u32 v6, v96, v97, v98\n" \
  "v_add3_u32 v7, v99, v112, v113\nv_mfma_f32_32x32x16_bf16 v[128:143], v[116:119], v[192:195], v[128:143]\nv_add3_u32 v8, v114, v115, v70\nv_add3_u32 v6, v6, v7, v8\n" \
  "v_exp_f32 v170, v170\nv_exp_f32 v171, v171\nv_add_u32_dpp v6, v6, v6 quad_perm:[1,0,3,2] row_mask:0xf bank_mask:0xf\nv_exp_f32 v172, v172\nv_exp_f32 v173, v173\n" \
  "v_add_u32_dpp v6, v6, v6 quad_perm:[2,3,0,1] row_mask:0xf bank_mask:0xf\nv_exp_f32 v174, v174\nv_exp_f32 v175, v175\nv_cmp_ne_u32 vcc, s36, v6\n" \
  "v_cvt_pk_bf16_f32 v196, v168, v169\nv_cvt_pk_bf16_f32 v197, v170, v171\nv_cvt_pk_bf16_f32 v198, v172, v173\nv_cvt_pk_bf16_f32 v199, v174, v175\ns_cbranch_scc1 .Lfz_xb2\n" \
  "s_cbranch_vccnz .Lfz_rb2\n.Lfz_kb2:\nv_mfma_f32_32x32x16_bf16 v[128:143], v[60:63], v[196:199], v[128:143]\nv_permlane16_swap_b32 v112, v56\n" \
  "v_permlane16_swap_b32 v113, v57\nv_permlane16_swap_b32 v114, v58\nv_permlane16_swap_b32 v115, v59\nv_exp_f32 v176, v176\nv_exp_f32 v177, v177\n" \
  "v_mfma_f32_32x32x16_bf16 v[160:175], v[96:99], v[208:211], 0\nv_exp_f32 v178, v178\nv_exp_f32 v179, v179\nv_exp_f32 v180, v180\nv_exp_f32 v181, v181\ns_waitcnt vmcnt(4)\n" \
  "v_bitop3_b32 v6, v225, v227, s48 bitop3:0x7e\nv_bitop3_b32 v7, v229, v231, s48 bitop3:0x7e\nv_max3_u32 v8, v224, v226, v228\nv_or_b32 v6, v6, v7\nv_max_u32 v8, v8, v230\n" \
  "v_cmp_ne_u32 vcc, 0, v6\ns_cbranch_vccnz .Lfz_gs\n.Lfz_gsr:\nv_exp_f32 v182, v182\nv_max_u32_dpp v8, v8, v8 quad_perm:[1,0,3,2] row_mask:0xf bank_mask:0xf\n" \
  "v_exp_f32 v183, v183\nv_exp_f32 v184, v184\nv_max_u32_dpp v8, v8, v8 quad_perm:[2,3,0,1] row_mask:0xf bank_mask:0xf\nv_exp_f32 v185, v185\n" \
  "v_cvt_pk_bf16_f32 v200, v176, v177\nv_max_u32_dpp v8, v8, v8 row_half_mirror row_mask:0xf bank_mask:0xf\nv_cvt_pk_bf16_f32 v201, v178, v179\n" \
  "v_cvt_pk_bf16_f32 v202, v180, v181\nv_max_u32_dpp v8, v8, v8 row_mirror row_mask:0xf bank_mask:0xf\nv_cvt_pk_bf16_f32 v203, v182, v183\nv_readlane_b32 s40, v8, 0\n" \
  "v_readlane_b32 s41, v8, 16\nv_readlane_b32 s42, v8, 32\nv_readlane_b32 s43, v8, 48\ns_max_u32 s40, s40, s41\ns_max_u32 s42, s42, s43\ns_max_u32 s10, s40, s42\n" \
  "s_andn2_b32 s40, 2047, s10\ns_lshl_b32 s40, s40, 10\ns_lshl_b32 s41, s15, 7\ns_add_u32 s40, s40, s41\ns_add_u32 s40, s8, s40\ns_addc_u32 s41, s9, 0\n" \
  "v_lshrrev_b32 v9, 1, v1\nv_and_b32 v9, 16, v9\ns_mov_b32 exec_lo, -3\ns_mov_b32 exec_hi, -3\nglobal_load_dwordx4 v[232:235], v9, s[40:41] offset:0\n" \
  "global_load_dwordx4 v[236:239], v9, s[40:41] offset:32\nglobal_load_dwordx4 v[240:243], v9, s[40:41] offset:64\nglobal_load_dwordx4 v[244:247], v9, s[40:41] offset:96\n" \
  "s_mov_b64 exec, -1\nv_exp_f32 v186, v186\nv_exp_f32 v187, v187\nv_mfma_f32_32x32x16_bf16 v[144:159], v[112:115], v[200:203], v[144:159]\nv_exp_f32 v188, v188\n" \
  "v_exp_f32 v189, v189\nv_exp_f32 v190, v190\nv_exp_f32 v191, v191\ns_nop 0\nv_cvt_pk_bf16_f32 v204, v184, v185\nv_cvt_pk_bf16_f32 v205, v186, v187\n" \
  "v_cvt_pk_bf16_f32 v206, v188, v189\nv_cvt_pk_bf16_f32 v207, v190, v191\nv_exp_f32 v160, v160\nv_exp_f32 v161, v161\n" \
  "v_mfma_f32_32x32x16_bf16 v[144:159], v[56:59], v[204:207], v[144:159]\nv_exp_f32 v162, v162\nv_exp_f32 v163, v163\nv_exp_f32 v164, v164\nv_exp_f32 v165, v165\n" \
  "s_cmp_lg_u32 s11, 0\ns_cbranch_scc1 .Lfz_wdb4\n.Lfz_wdrb4:\ns_add_u32 s40, s31, 0\ns_and_b32 s40, s40, 7\ns_lshl_b32 s40, s40, 16\ns_add_u32 m0, s3, 0\n" \
  "s_add_u32 s40, s12, s40\ns_addc_u32 s41, s13, 0\nglobal_load_lds_dwordx4 v10, s[40:41]\ns_add_u32 m0, s3, 4096\ns_add_u32 s40, s40, 32768\ns_addc_u32 s41, s41, 0\n" \
  "global_load_lds_dwordx4 v10, s[40:41]\ns_add_u32 s40, s31, 1\ns_and_b32 s40, s40, 7\ns_lshl_b32 s40, s40, 16\ns_add_u32 m0, s3, 8192\ns_add_u32 s40, s12, s40\n" \
  "s_addc_u32 s41, s13, 0\nglobal_load_lds_dwordx4 v10, s[40:41]\ns_add_u32 m0, s3, 12288\ns_add_u32 s40, s40, 32768\ns_addc_u32 s41, s41, 0\n" \
  "global_load_lds_dwordx4 v10, s[40:41]\nv_exp_f32 v166, v166\nv_exp_f32 v167, v167\nv_exp_f32 v168, v168\nv_exp_f32 v169, v169\nv_cvt_pk_bf16_f32 v192, v160, v161\n" \
  "v_cvt_pk_bf16_f32 v193, v162, v163\nv_cvt_pk_bf16_f32 v194, v164, v165\nv_cvt_pk_bf16_f32 v195, v166, v167\ns_cmp_lt_i32 s32, -1\ns_cbranch_scc1 .Lfz_wb4\n" \
  "s_waitcnt vmcnt(17)\n.Lfz_wb4:\nv_mfma_f32_32x32x16_bf16 v[176:191], v[92:95], v[212:215], 0\nv_add3_u32 v6, v92, v93, v94\nv_add3_u32 v7, v95, v108, v109\n" \
  "v_mfma_f32_32x32x16_bf16 v[128:143], v[112:115], v[192:195], v[128:143]\nv_add3_u32 v8, v110, v111, v69\nv_add3_u32 v6, v6, v7, v8\nv_exp_f32 v170, v170\n" \
  "v_exp_f32 v171, v171\nv_add_u32_dpp v6, v6, v6 quad_perm:[1,0,3,2] row_mask:0xf bank_mask:0xf\nv_exp_f32 v172, v172\nv_exp_f32 v173, v173\n" \
  "v_add_u32_dpp v6, v6, v6 quad_perm:[2,3,0,1] row_mask:0xf bank_mask:0xf\nv_exp_f32 v174, v174\nv_exp_f32 v175, v175\nv_cmp_ne_u32 vcc, s36, v6\n" \
  "v_cvt_pk_bf16_f32 v196, v168, v169\nv_cvt_pk_bf16_f32 v197, v170, v171\nv_cvt_pk_bf16_f32 v198, v172, v173\nv_cvt_pk_bf16_f32 v199, v174, v175\ns_cbranch_scc1 .Lfz_xb4\n" \
  "s_cbranch_vccnz .Lfz_rb4\n.Lfz_kb4:\nv_mfma_f32_32x32x16_bf16 v[128:143], v[56:59], v[196:199], v[128:143]\nv_permlane16_swap_b32 v108, v52\n" \
  "v_permlane16_swap_b32 v109, v53\nv_permlane16_swap_b32 v110, v54\nv_permlane16_swap_b32 v111, v55\nv_exp_f32 v176, v176\nv_exp_f32 v177, v177\n" \
  "v_mfma_f32_32x32x16_bf16 v[160:175], v[92:95], v[208:211], 0\nv_exp_f32 v178, v178\nv_exp_f32 v179, v179\nv_exp_f32 v180, v180\nv_exp_f32 v181, v181\n" \
  "s_add_u32 s40, s31, 2\ns_and_b32 s40, s40, 7\ns_lshl_b32 s40, s40, 16\ns_add_u32 m0, s3, 16384\ns_add_u32 s40, s12, s40\ns_addc_u32 s41, s13, 0\n" \
  "global_load_lds_dwordx4 v10, s[40:41]\ns_add_u32 m0, s3, 20480\ns_add_u32 s40, s40, 32768\ns_addc_u32 s41, s41, 0\nglobal_load_lds_dwordx4 v10, s[40:41]\n" \
  "s_add_u32 s40, s31, 3\ns_and_b32 s40, s40, 7\ns_lshl_b32 s40, s40, 16\ns_add_u32 m0, s3, 24576\ns_add_u32 s40, s12, s40\ns_addc_u32 s41, s13, 0\n" \
  "global_load_lds_dwordx4 v10, s[40:41]\ns_add_u32 m0, s3, 28672\ns_add_u32 s40, s40, 32768\ns_addc_u32 s41, s41, 0\nglobal_load_lds_dwordx4 v10, s[40:41]\n" \
  "v_exp_f32 v182, v182\nv_exp_f32 v183, v183\nv_exp_f32 v184, v184\nv_exp_f32 v185, v185\nv_cvt_pk_bf16_f32 v200, v176, v177\nv_cvt_pk_bf16_f32 v201, v178, v179\n" \
  "v_cvt_pk_bf16_f32 v202, v180, v181\nv_cvt_pk_bf16_f32 v203, v182, v183\nv_exp_f32 v186, v186\nv_exp_f32 v187, v187\n" \
  "v_mfma_f32_32x32x16_bf16 v[144:159], v[108:111], v[200:203], v[144:159]\nv_exp_f32 v188, v188\nv_exp_f32 v189, v189\nv_exp_f32 v190, v190\nv_exp_f32 v191, v191\ns_nop 0\n" \
  "v_cvt_pk_bf16_f32 v204, v184, v185\nv_cvt_pk_bf16_f32 v205, v186, v187\nv_cvt_pk_bf16_f32 v206, v188, v189\nv_cvt_pk_bf16_f32 v207, v190, v191\nv_exp_f32 v160, v160\n" \
  "v_exp_f32 v161, v161\nv_mfma_f32_32x32x16_bf16 v[144:159], v[52:55], v[204:207], v[144:159]\nv_exp_f32 v162, v162\nv_exp_f32 v163, v163\nv_exp_f32 v164, v164\n" \
  "v_exp_f32 v165, v165\ns_add_u32 s40, s31, 4\ns_and_b32 s40, s40, 7\ns_lshl_b32 s40, s40, 16\ns_add_u32 m0, s3, 32768\ns_add_u32 s40, s12, s40\ns_addc_u32 s41, s13, 0\n" \
  "global_load_lds_dwordx4 v10, s[40:41]\ns_add_u32 m0, s3, 36864\ns_add_u32 s40, s40, 32768\ns_addc_u32 s41, s41, 0\nglobal_load_lds_dwordx4 v10, s[40:41]\n" \
  "s_add_u32 s40, s31, 5\ns_and_b32 s40, s40, 7\ns_lshl_b32 s40, s40, 16\ns_add_u32 m0, s3, 40960\ns_add_u32 s40, s12, s40\ns_addc_u32 s41, s13, 0\n" \
  "global_load_lds_dwordx4 v10, s[40:41]\ns_add_u32 m0, s3, 45056\ns_add_u32 s40, s40, 32768\ns_addc_u32 s41, s41, 0\nglobal_load_lds_dwordx4 v10, s[40:41]\n" \
  "v_exp_f32 v166, v166\nv_exp_f32 v167, v167\nv_exp_f32 v168, v168\nv_exp_f32 v169, v169\nv_cvt_pk_bf16_f32 v192, v160, v161\nv_cvt_pk_bf16_f32 v193, v162, v163\n" \
  "v_cvt_pk_bf16_f32 v194, v164, v165\nv_cvt_pk_bf16_f32 v195, v166, v167\ns_cmp_lt_i32 s32, 0\ns_cbranch_scc1 .Lfz_wb6\ns_waitcnt vmcnt(22)\n.Lfz_wb6:\n" \
  "v_mfma_f32_32x32x16_bf16 v[176:191], v[88:91], v[212:215], 0\nv_add3_u32 v6, v88, v89, v90\nv_add3_u32 v7, v91, v104, v105\n" \
  "v_mfma_f32_32x32x16_bf16 v[128:143], v[108:111], v[192:195], v[128:143]\nv_add3_u32 v8, v106, v107, v68\nv_add3_u32 v6, v6, v7, v8\nv_exp_f32 v170, v170\n" \
  "v_exp_f32 v171, v171\nv_add_u32_dpp v6, v6, v6 quad_perm:[1,0,3,2] row_mask:0xf bank_mask:0xf\nv_exp_f32 v172, v172\nv_exp_f32 v173, v173\n" \
  "v_add_u32_dpp v6, v6, v6 quad_perm:[2,3,0,1] row_mask:0xf bank_mask:0xf\nv_exp_f32 v174, v174\nv_exp_f32 v175, v175\nv_cmp_ne_u32 vcc, s36, v6\n" \
  "v_cvt_pk_bf16_f32 v196, v168, v169\nv_cvt_pk_bf16_f32 v197, v170, v171\nv_cvt_pk_bf16_f32 v198, v172, v173\nv_cvt_pk_bf16_f32 v199, v174, v175\ns_cbranch_scc1 .Lfz_xb6\n" \
  "s_cbranch_vccnz .Lfz_rb6\n.Lfz_kb6:\nv_mfma_f32_32x32x16_bf16 v[128:143], v[52:55], v[196:199], v[128:143]\nv_permlane16_swap_b32 v104, v48\n" \
  "v_permlane16_swap_b32 v105, v49\nv_permlane16_swap_b32 v106, v50\nv_permlane16_swap_b32 v107, v51\nv_exp_f32 v176, v176\nv_exp_f32 v177, v177\n" \
  "v_mfma_f32_32x32x16_bf16 v[160:175], v[88:91], v[208:211], v[16:31]\nv_exp_f32 v178, v178\nv_exp_f32 v179, v179\nv_exp_f32 v180, v180\nv_exp_f32 v181, v181\n" \
  "s_add_u32 s40, s31, 6\ns_and_b32 s40, s40, 7\ns_lshl_b32 s40, s40, 16\ns_add_u32 m0, s3, 49152\ns_add_u32 s40, s12, s40\ns_addc_u32 s41, s13, 0\n" \
  "global_load_lds_dwordx4 v10, s[40:41]\ns_add_u32 m0, s3, 53248\ns_add_u32 s40, s40, 32768\ns_addc_u32 s41, s41, 0\nglobal_load_lds_dwordx4 v10, s[40:41]\n" \
  "s_add_u32 s40, s31, 7\ns_and_b32 s40, s40, 7\ns_lshl_b32 s40, s40, 16\ns_add_u32 m0, s3, 57344\ns_add_u32 s40, s12, s40\ns_addc_u32 s41, s13, 0\n" \
  "global_load_lds_dwordx4 v10, s[40:41]\ns_add_u32 m0, s3, 61440\ns_add_u32 s40, s40, 32768\ns_addc_u32 s41, s41, 0\nglobal_load_lds_dwordx4 v10, s[40:41]\n" \
  "s_lshl_b32 s40, s1, 16\ns_lshl_b32 s41, s2, 7\ns_add_u32 s40, s40, s41\ns_add_u32 s40, s8, s40\ns_addc_u32 s41, s9, 0\nglobal_load_dwordx4 v[216:219], v12, s[40:41]\n" \
  "global_load_dwordx4 v[220:223], v15, s[60:61] sc1\nv_exp_f32 v182, v182\nv_exp_f32 v183, v183\nv_exp_f32 v184, v184\nv_exp_f32 v185, v185\n" \
  "v_cvt_pk_bf16_f32 v200, v176, v177\nv_cvt_pk_bf16_f32 v201, v178, v179\nv_cvt_pk_bf16_f32 v202, v180, v181\nv_cvt_pk_bf16_f32 v203, v182, v183\nv_exp_f32 v186, v186\n" \
  "v_exp_f32 v187, v187\nv_mfma_f32_32x32x16_bf16 v[144:159], v[104:107], v[200:203], v[144:159]\nv_exp_f32 v188, v188\nv_exp_f32 v189, v189\nv_exp_f32 v190, v190\n" \
  "v_exp_f32 v191, v191\ns_nop 0\nv_cvt_pk_bf16_f32 v204, v184, v185\nv_cvt_pk_bf16_f32 v205, v186, v187\nv_cvt_pk_bf16_f32 v206, v188, v189\n" \
  "v_cvt_pk_bf16_f32 v207, v190, v191\nv_exp_f32 v160, v160\nv_exp_f32 v161, v161\nv_mfma_f32_32x32x16_bf16 v[144:159], v[48:51], v[204:207], v[144:159]\n" \
  "v_exp_f32 v162, v162\nv_exp_f32 v163, v163\nv_exp_f32 v164, v164\nv_exp_f32 v165, v165\ns_waitcnt vmcnt(18)\ns_cmp_lt_u32 s18, 43\ns_cbranch_scc0 .Lfz_ae\n" \
  "v_add_u32 v9, s19, v2\nds_write_b128 v9, v[32:35] offset:0\nds_write_b128 v9, v[36:39] offset:1024\nds_write_b128 v9, v[40:43] offset:2048\n" \
  "ds_write_b128 v9, v[44:47] offset:3072\nv_lshrrev_b32 v6, 5, v1\nv_bfe_u32 v7, v1, 2, 2\nv_lshl_or_b32 v6, v6, 3, v7\nv_bfe_u32 v7, v1, 4, 1\n" \
  "v_lshl_or_b32 v6, v6, 1, v7\nv_and_b32 v7, 3, v1\nv_lshl_or_b32 v6, v6, 2, v7\nv_lshl_add_u32 v6, v6, 3, s19\nds_read_b64_tr_b16 v[224:225], v6 offset:0\n" \
  "ds_read_b64_tr_b16 v[226:227], v6 offset:256\nds_read_b64_tr_b16 v[228:229], v6 offset:1024\nds_read_b64_tr_b16 v[230:231], v6 offset:1280\n" \
  "ds_read_b64_tr_b16 v[248:249], v6 offset:2048\nds_read_b64_tr_b16 v[250:251], v6 offset:2304\nds_read_b64_tr_b16 v[252:253], v6 offset:3072\n" \
  "ds_read_b64_tr_b16 v[254:255], v6 offset:3328\ns_lshl_b32 s40, s18, 13\ns_lshr_b32 s41, s27, 1\ns_lshl_b32 s41, s41, 12\ns_add_u32 s40, s40, s41\ns_lshl_b32 s41, s15, 9\n" \
  "s_add_u32 s40, s40, s41\ns_add_u32 s40, s40, 65536\ns_add_u32 s40, s24, s40\ns_addc_u32 s41, s25, 0\ns_waitcnt lgkmcnt(6)\n" \
  "v_mfma_f32_32x32x16_bf16 v[32:47], v[232:235], v[224:227], 0\ns_waitcnt lgkmcnt(4)\nv_mfma_f32_32x32x16_bf16 v[32:47], v[236:239], v[228:231], v[32:47]\n" \
  "s_waitcnt lgkmcnt(2)\nv_mfma_f32_32x32x16_bf16 v[32:47], v[240:243], v[248:251], v[32:47]\ns_waitcnt lgkmcnt(0)\n" \
  "v_mfma_f32_32x32x16_bf16 v[32:47], v[244:247], v[252:255], v[32:47]\ns_nop 11\nv_mov_b32 v34, v33\nv_mov_b32 v33, s48\nv_mov_b32 v35, s48\ns_mov_b32 exec_hi, 0\n" \
  "global_store_dwordx4 v2, v[32:35], s[40:41] sc1\ns_mov_b64 exec, -1\n.Lfz_ae:\nv_exp_f32 v166, v166\nv_exp_f32 v167, v167\nv_exp_f32 v168, v168\nv_exp_f32 v169, v169\n" \
  "v_cvt_pk_bf16_f32 v192, v160, v161\nv_cvt_pk_bf16_f32 v193, v162, v163\nv_cvt_pk_bf16_f32 v194, v164, v165\nv_cvt_pk_bf16_f32 v195, v166, v167\n" \
  "ds_write_b128 v14, v[144:147] offset:57344\nds_write_b128 v14, v[148:151] offset:58368\nds_write_b32 v86, v152\nv_exp_f32 v170, v170\nv_exp_f32 v171, v171\n" \
  "v_mfma_f32_32x32x16_bf16 v[128:143], v[104:107], v[192:195], v[128:143]\nv_exp_f32 v172, v172\nv_exp_f32 v173, v173\nv_exp_f32 v174, v174\nv_exp_f32 v175, v175\ns_nop 0\n" \
  "v_cvt_pk_bf16_f32 v196, v168, v169\nv_cvt_pk_bf16_f32 v197, v170, v171\nv_cvt_pk_bf16_f32 v198, v172, v173\nv_cvt_pk_bf16_f32 v199, v174, v175\ns_nop 1\n" \
  "v_mfma_f32_32x32x16_bf16 v[128:143], v[48:51], v[196:199], v[128:143]\ns_waitcnt vmcnt(1)\ns_cmp_lt_u32 s18, 43\ns_cbranch_scc1 .Lfz_ae2\ns_waitcnt vmcnt(0)\n.Lfz_ae2:\n" \
  "v_mov_b32 v6, 155664\nv_mov_b32 v7, s10\ns_mov_b64 exec, 1\nds_write_b32 v6, v7\ns_mov_b64 exec, -1\ns_branch .Lfz_comb\n.Lfz_s1:\ns_cmp_ge_i32 s32, -3\n" \
  "s_cbranch_scc0 .Lfz_dm5\nglobal_load_dwordx4 v[100:103], v2, s[58:59] sc1\nglobal_load_dwordx4 v[116:119], v2, s[58:59] offset:1024 sc1\ns_mov_b64 exec, s[50:51]\n" \
  "global_load_dword v71, v3, s[58:59] sc1\n.Lfz_dr5:\ns_mov_b64 exec, -1\ns_cmp_ge_i32 s32, -2\ns_cbranch_scc0 .Lfz_dm6\nglobal_load_dwordx4 v[96:99], v2, s[56:57] sc1\n" \
  "global_load_dwordx4 v[112:115], v2, s[56:57] offset:1024 sc1\ns_mov_b64 exec, s[50:51]\nglobal_load_dword v70, v3, s[56:57] sc1\n.Lfz_dr6:\ns_mov_b64 exec, -1\n" \
  "s_cmp_ge_i32 s32, -1\ns_cbranch_scc0 .Lfz_dm7\nglobal_load_dwordx4 v[92:95], v2, s[54:55] sc1\nglobal_load_dwordx4 v[108:111], v2, s[54:55] offset:1024 sc1\n" \
  "s_mov_b64 exec, s[50:51]\nglobal_load_dword v69, v3, s[54:55] sc1\n.Lfz_dr7:\ns_mov_b64 exec, -1\ns_cmp_ge_i32 s32, 0\ns_cbranch_scc0 .Lfz_dm8\n" \
  "global_load_dwordx4 v[88:91], v2, s[52:53] sc1\nglobal_load_dwordx4 v[104:107], v2, s[52:53] offset:1024 sc1\ns_mov_b64 exec, s[50:51]\n" \
  "global_load_dword v68, v3, s[52:53] sc1\n.Lfz_dr8:\ns_mov_b64 exec, -1\ns_waitcnt lgkmcnt(2)\nv_mfma_f32_32x32x16_bf16 v[160:175], v[120:123], v[208:211], v[16:31]\n" \
  "s_waitcnt lgkmcnt(0)\nv_permlane16_swap_b32 v124, v64\nv_permlane16_swap_b32 v125, v65\nv_permlane16_swap_b32 v126, v66\nv_permlane16_swap_b32 v127, v67\ns_nop 6\n" \
  "v_exp_f32 v160, v160\nv_exp_f32 v161, v161\nv_mfma_f32_32x32x16_bf16 v[176:191], v[120:123], v[212:215], 0\nv_exp_f32 v162, v162\nv_exp_f32 v163, v163\n" \
  "v_exp_f32 v164, v164\nv_exp_f32 v165, v165\ns_cmp_lg_u32 s11, 0\ns_cbranch_scc1 .Lfz_wda0\n.Lfz_wdra0:\nv_exp_f32 v166, v166\nv_exp_f32 v167, v167\nv_exp_f32 v168, v168\n" \
  "v_exp_f32 v169, v169\nv_cvt_pk_bf16_f32 v192, v160, v161\nv_cvt_pk_bf16_f32 v193, v162, v163\nv_cvt_pk_bf16_f32 v194, v164, v165\nv_cvt_pk_bf16_f32 v195, v166, v167\n" \
  "v_exp_f32 v170, v170\nv_exp_f32 v171, v171\nv_mfma_f32_32x32x16_bf16 v[128:143], v[124:127], v[192:195], 0\nv_exp_f32 v172, v172\nv_exp_f32 v173, v173\n" \
  "v_exp_f32 v174, v174\nv_exp_f32 v175, v175\ns_nop 0\nv_cvt_pk_bf16_f32 v196, v168, v169\nv_cvt_pk_bf16_f32 v197, v170, v171\nv_cvt_pk_bf16_f32 v198, v172, v173\n" \
  "v_cvt_pk_bf16_f32 v199, v174, v175\nv_exp_f32 v176, v176\nv_exp_f32 v177, v177\nv_mfma_f32_32x32x16_bf16 v[128:143], v[64:67], v[196:199], v[128:143]\n" \
  "v_exp_f32 v178, v178\nv_exp_f32 v179, v179\nv_exp_f32 v180, v180\nv_exp_f32 v181, v181\nv_xor_b32 v16, 0xff800000, v16\nv_xor_b32 v17, 0xff800000, v17\n" \
  "v_xor_b32 v18, 0xff800000, v18\nv_xor_b32 v19, 0xff800000, v19\nv_xor_b32 v20, 0xff800000, v20\nv_xor_b32 v21, 0xff800000, v21\nv_xor_b32 v22, 0xff800000, v22\n" \
  "v_xor_b32 v23, 0xff800000, v23\nv_xor_b32 v24, 0xff800000, v24\nv_xor_b32 v25, 0xff800000, v25\nv_xor_b32 v26, 0xff800000, v26\nv_xor_b32 v27, 0xff800000, v27\n" \
  "v_xor_b32 v28, 0xff800000, v28\nv_xor_b32 v29, 0xff800000, v29\nv_xor_b32 v30, 0xff800000, v30\nv_xor_b32 v31, 0xff800000, v31\nv_exp_f32 v182, v182\n" \
  "v_exp_f32 v183, v183\nv_exp_f32 v184, v184\nv_exp_f32 v185, v185\nv_cvt_pk_bf16_f32 v200, v176, v177\nv_cvt_pk_bf16_f32 v201, v178, v179\n" \
  "v_cvt_pk_bf16_f32 v202, v180, v181\nv_cvt_pk_bf16_f32 v203, v182, v183\ns_cmp_lt_i32 s32, -3\ns_cbranch_scc1 .Lfz_wa1\ns_waitcnt vmcnt(9)\n.Lfz_wa1:\n" \
  "v_mfma_f32_32x32x16_bf16 v[160:175], v[100:103], v[208:211], 0\nv_add3_u32 v6, v100, v101, v102\nv_add3_u32 v7, v103, v116, v117\n" \
  "v_mfma_f32_32x32x16_bf16 v[144:159], v[124:127], v[200:203], 0\nv_add3_u32 v8, v118, v119, v71\nv_add3_u32 v6, v6, v7, v8\nv_exp_f32 v186, v186\nv_exp_f32 v187, v187\n" \
  "v_add_u32_dpp v6, v6, v6 quad_perm:[1,0,3,2] row_mask:0xf bank_mask:0xf\nv_exp_f32 v188, v188\nv_exp_f32 v189, v189\n" \
  "v_add_u32_dpp v6, v6, v6 quad_perm:[2,3,0,1] row_mask:0xf bank_mask:0xf\nv_exp_f32 v190, v190\nv_exp_f32 v191, v191\nv_cmp_ne_u32 vcc, s36, v6\n" \
  "v_cvt_pk_bf16_f32 v204, v184, v185\nv_cvt_pk_bf16_f32 v205, v186, v187\nv_cvt_pk_bf16_f32 v206, v188, v189\nv_cvt_pk_bf16_f32 v207, v190, v191\ns_cbranch_scc1 .Lfz_xa1\n" \
  "s_cbranch_vccnz .Lfz_ra1\n.Lfz_ka1:\nv_mfma_f32_32x32x16_bf16 v[144:159], v[64:67], v[204:207], v[144:159]\nv_permlane16_swap_b32 v116, v60\n" \
  "v_permlane16_swap_b32 v117, v61\nv_permlane16_swap_b32 v118, v62\nv_permlane16_swap_b32 v119, v63\nv_exp_f32 v160, v160\nv_exp_f32 v161, v161\n" \
  "v_mfma_f32_32x32x16_bf16 v[176:191], v[100:103], v[212:215], 0\nv_exp_f32 v162, v162\nv_exp_f32 v163, v163\nv_exp_f32 v164, v164\nv_exp_f32 v165, v165\n" \
  "s_cmp_lg_u32 s11, 0\ns_cbranch_scc1 .Lfz_wda2\n.Lfz_wdra2:\nv_exp_f32 v166, v166\nv_exp_f32 v167, v167\nv_exp_f32 v168, v168\nv_exp_f32 v169, v169\n" \
  "v_cvt_pk_bf16_f32 v192, v160, v161\nv_cvt_pk_bf16_f32 v193, v162, v163\nv_cvt_pk_bf16_f32 v194, v164, v165\nv_cvt_pk_bf16_f32 v195, v166, v167\nv_exp_f32 v170, v170\n" \
  "v_exp_f32 v171, v171\nv_mfma_f32_32x32x16_bf16 v[128:143], v[116:119], v[192:195], v[128:143]\nv_exp_f32 v172, v172\nv_exp_f32 v173, v173\nv_exp_f32 v174, v174\n" \
  "v_exp_f32 v175, v175\ns_nop 0\nv_cvt_pk_bf16_f32 v196, v168, v169\nv_cvt_pk_bf16_f32 v197, v170, v171\nv_cvt_pk_bf16_f32 v198, v172, v173\n" \
  "v_cvt_pk_bf16_f32 v199, v174, v175\nv_exp_f32 v176, v176\nv_exp_f32 v177, v177\nv_mfma_f32_32x32x16_bf16 v[128:143], v[60:63], v[196:199], v[128:143]\n" \
  "v_exp_f32 v178, v178\nv_exp_f32 v179, v179\nv_exp_f32 v180, v180\nv_exp_f32 v181, v181\nv_exp_f32 v182, v182\nv_exp_f32 v183, v183\nv_exp_f32 v184, v184\n" \
  "v_exp_f32 v185, v185\nv_cvt_pk_bf16_f32 v200, v176, v177\nv_cvt_pk_bf16_f32 v201, v178, v179\nv_cvt_pk_bf16_f32 v202, v180, v181\nv_cvt_pk_bf16_f32 v203, v182, v183\n" \
  "s_cmp_lt_i32 s32, -2\ns_cbranch_scc1 .Lfz_wa3\ns_waitcnt vmcnt(6)\n.Lfz_wa3:\nv_mfma_f32_32x32x16_bf16 v[160:175], v[96:99], v[208:211], 0\nv_add3_u32 v6, v96, v97, v98\n" \
  "v_add3_u32 v7, v99, v112, v113\nv_mfma_f32_32x32x16_bf16 v[144:159], v[116:119], v[200:203], v[144:159]\nv_add3_u32 v8, v114, v115, v70\nv_add3_u32 v6, v6, v7, v8\n" \
  "v_exp_f32 v186, v186\nv_exp_f32 v187, v187\nv_add_u32_dpp v6, v6, v6 quad_perm:[1,0,3,2] row_mask:0xf bank_mask:0xf\nv_exp_f32 v188, v188\nv_exp_f32 v189, v189\n" \
  "v_add_u32_dpp v6, v6, v6 quad_perm:[2,3,0,1] row_mask:0xf bank_mask:0xf\nv_exp_f32 v190, v190\nv_exp_f32 v191, v191\nv_cmp_ne_u32 vcc, s36, v6\n" \
  "v_cvt_pk_bf16_f32 v204, v184, v185\nv_cvt_pk_bf16_f32 v205, v186, v187\nv_cvt_pk_bf16_f32 v206, v188, v189\nv_cvt_pk_bf16_f32 v207, v190, v191\ns_cbranch_scc1 .Lfz_xa3\n" \
  "s_cbranch_vccnz .Lfz_ra3\n.Lfz_ka3:\nv_mfma_f32_32x32x16_bf16 v[144:159], v[60:63], v[204:207], v[144:159]\nv_permlane16_swap_b32 v112, v56\n" \
  "v_permlane16_swap_b32 v113, v57\nv_permlane16_swap_b32 v114, v58\nv_permlane16_swap_b32 v115, v59\nv_exp_f32 v160, v160\nv_exp_f32 v161, v161\n" \
  "v_mfma_f32_32x32x16_bf16 v[176:191], v[96:99], v[212:215], 0\nv_exp_f32 v162, v162\nv_exp_f32 v163, v163\nv_exp_f32 v164, v164\nv_exp_f32 v165, v165\n" \
  "s_cmp_lg_u32 s11, 0\ns_cbranch_scc1 .Lfz_wda4\n.Lfz_wdra4:\nv_exp_f32 v166, v166\nv_exp_f32 v167, v167\nv_exp_f32 v168, v168\nv_exp_f32 v169, v169\n" \
  "v_cvt_pk_bf16_f32 v192, v160, v161\nv_cvt_pk_bf16_f32 v193, v162, v163\nv_cvt_pk_bf16_f32 v194, v164, v165\nv_cvt_pk_bf16_f32 v195, v166, v167\nv_exp_f32 v170, v170\n" \
  "v_exp_f32 v171, v171\nv_mfma_f32_32x32x16_bf16 v[128:143], v[112:115], v[192:195], v[128:143]\nv_exp_f32 v172, v172\nv_exp_f32 v173, v173\nv_exp_f32 v174, v174\n" \
  "v_exp_f32 v175, v175\ns_nop 0\nv_cvt_pk_bf16_f32 v196, v168, v169\nv_cvt_pk_bf16_f32 v197, v170, v171\nv_cvt_pk_bf16_f32 v198, v172, v173\n" \
  "v_cvt_pk_bf16_f32 v199, v174, v175\nv_exp_f32 v176, v176\nv_exp_f32 v177, v177\nv_mfma_f32_32x32x16_bf16 v[128:143], v[56:59], v[196:199], v[128:143]\n" \
  "v_exp_f32 v178, v178\nv_exp_f32 v179, v179\nv_exp_f32 v180, v180\nv_exp_f32 v181, v181\nv_exp_f32 v182, v182\nv_exp_f32 v183, v183\nv_exp_f32 v184, v184\n" \
  "v_exp_f32 v185, v185\nv_cvt_pk_bf16_f32 v200, v176, v177\nv_cvt_pk_bf16_f32 v201, v178, v179\nv_cvt_pk_bf16_f32 v202, v180, v181\nv_cvt_pk_bf16_f32 v203, v182, v183\n" \
  "s_cmp_lt_i32 s32, -1\ns_cbranch_scc1 .Lfz_wa5\ns_waitcnt vmcnt(3)\n.Lfz_wa5:\nv_mfma_f32_32x32x16_bf16 v[160:175], v[92:95], v[208:211], 0\nv_add3_u32 v6, v92, v93, v94\n" \
  "v_add3_u32 v7, v95, v108, v109\nv_mfma_f32_32x32x16_bf16 v[144:159], v[112:115], v[200:203], v[144:159]\nv_add3_u32 v8, v110, v111, v69\nv_add3_u32 v6, v6, v7, v8\n" \
  "v_exp_f32 v186, v186\nv_exp_f32 v187, v187\nv_add_u32_dpp v6, v6, v6 quad_perm:[1,0,3,2] row_mask:0xf bank_mask:0xf\nv_exp_f32 v188, v188\nv_exp_f32 v189, v189\n" \
  "v_add_u32_dpp v6, v6, v6 quad_perm:[2,3,0,1] row_mask:0xf bank_mask:0xf\nv_exp_f32 v190, v190\nv_exp_f32 v191, v191\nv_cmp_ne_u32 vcc, s36, v6\n" \
  "v_cvt_pk_bf16_f32 v204, v184, v185\nv_cvt_pk_bf16_f32 v205, v186, v187\nv_cvt_pk_bf16_f32 v206, v188, v189\nv_cvt_pk_bf16_f32 v207, v190, v191\ns_cbranch_scc1 .Lfz_xa5\n" \
  "s_cbranch_vccnz .Lfz_ra5\n.Lfz_ka5:\nv_mfma_f32_32x32x16_bf16 v[144:159], v[56:59], v[204:207], v[144:159]\nv_permlane16_swap_b32 v108, v52\n" \
  "v_permlane16_swap_b32 v109, v53\nv_permlane16_swap_b32 v110, v54\nv_permlane16_swap_b32 v111, v55\nv_exp_f32 v160, v160\nv_exp_f32 v161, v161\n" \
  "v_mfma_f32_32x32x16_bf16 v[176:191], v[92:95], v[212:215], 0\nv_exp_f32 v162, v162\nv_exp_f32 v163, v163\nv_exp_f32 v164, v164\nv_exp_f32 v165, v165\n" \
  "v_exp_f32 v166, v166\nv_exp_f32 v167, v167\nv_exp_f32 v168, v168\nv_exp_f32 v169, v169\nv_cvt_pk_bf16_f32 v192, v160, v161\nv_cvt_pk_bf16_f32 v193, v162, v163\n" \
  "v_cvt_pk_bf16_f32 v194, v164, v165\nv_cvt_pk_bf16_f32 v195, v166, v167\nv_exp_f32 v170, v170\nv_exp_f32 v171, v171\n" \
  "v_mfma_f32_32x32x16_bf16 v[128:143], v[108:111], v[192:195], v[128:143]\nv_exp_f32 v172, v172\nv_exp_f32 v173, v173\nv_exp_f32 v174, v174\nv_exp_f32 v175, v175\ns_nop 0\n" \
  "v_cvt_pk_bf16_f32 v196, v168, v169\nv_cvt_pk_bf16_f32 v197, v170, v171\nv_cvt_pk_bf16_f32 v198, v172, v173\nv_cvt_pk_bf16_f32 v199, v174, v175\nv_exp_f32 v176, v176\n" \
  "v_exp_f32 v177, v177\nv_mfma_f32_32x32x16_bf16 v[128:143], v[52:55], v[196:199], v[128:143]\nv_exp_f32 v178, v178\nv_exp_f32 v179, v179\nv_exp_f32 v180, v180\n" \
  "v_exp_f32 v181, v181\nv_exp_f32 v182, v182\nv_exp_f32 v183, v183\nv_exp_f32 v184, v184\nv_exp_f32 v185, v185\nv_cvt_pk_bf16_f32 v200, v176, v177\n" \
  "v_cvt_pk_bf16_f32 v201, v178, v179\nv_cvt_pk_bf16_f32 v202, v180, v181\nv_cvt_pk_bf16_f32 v203, v182, v183\ns_cmp_lt_i32 s32, 0\ns_cbranch_scc1 .Lfz_wa7\n" \
  "s_waitcnt vmcnt(0)\n.Lfz_wa7:\nv_mfma_f32_32x32x16_bf16 v[160:175], v[88:91], v[208:211], v[16:31]\nv_add3_u32 v6, v88, v89, v90\nv_add3_u32 v7, v91, v104, v105\n" \
  "v_mfma_f32_32x32x16_bf16 v[144:159], v[108:111], v[200:203], v[144:159]\nv_add3_u32 v8, v106, v107, v68\nv_add3_u32 v6, v6, v7, v8\nv_exp_f32 v186, v186\n" \
  "v_exp_f32 v187, v187\nv_add_u32_dpp v6, v6, v6 quad_perm:[1,0,3,2] row_mask:0xf bank_mask:0xf\nv_exp_f32 v188, v188\nv_exp_f32 v189, v189\n" \
  "v_add_u32_dpp v6, v6, v6 quad_perm:[2,3,0,1] row_mask:0xf bank_mask:0xf\nv_exp_f32 v190, v190\nv_exp_f32 v191, v191\nv_cmp_ne_u32 vcc, s36, v6\n" \
  "v_cvt_pk_bf16_f32 v204, v184, v185\nv_cvt_pk_bf16_f32 v205, v186, v187\nv_cvt_pk_bf16_f32 v206, v188, v189\nv_cvt_pk_bf16_f32 v207, v190, v191\ns_cbranch_scc1 .Lfz_xa7\n" \
  "s_cbranch_vccnz .Lfz_ra7\n.Lfz_ka7:\nv_mfma_f32_32x32x16_bf16 v[144:159], v[52:55], v[204:207], v[144:159]\nv_permlane16_swap_b32 v104, v48\n" \
  "v_permlane16_swap_b32 v105, v49\nv_permlane16_swap_b32 v106, v50\nv_permlane16_swap_b32 v107, v51\nv_exp_f32 v160, v160\nv_exp_f32 v161, v161\nv_exp_f32 v162, v162\n" \
  "v_exp_f32 v163, v163\nv_exp_f32 v164, v164\nv_exp_f32 v165, v165\ns_lshl_b32 s40, s1, 16\ns_lshl_b32 s41, s2, 7\ns_add_u32 s40, s40, s41\ns_add_u32 s40, s8, s40\n" \
  "s_addc_u32 s41, s9, 0\nglobal_load_dwordx4 v[216:219], v12, s[40:41]\nglobal_load_dwordx4 v[220:223], v15, s[60:61] sc1\nv_exp_f32 v166, v166\nv_exp_f32 v167, v167\n" \
  "v_exp_f32 v168, v168\nv_exp_f32 v169, v169\nv_cvt_pk_bf16_f32 v192, v160, v161\nv_cvt_pk_bf16_f32 v193, v162, v163\nv_cvt_pk_bf16_f32 v194, v164, v165\n" \
  "v_cvt_pk_bf16_f32 v195, v166, v167\nds_write_b128 v14, v[144:147] offset:57344\nds_write_b128 v14, v[148:151] offset:58368\nds_write_b32 v86, v152\nv_exp_f32 v170, v170\n" \
  "v_exp_f32 v171, v171\nv_mfma_f32_32x32x16_bf16 v[128:143], v[104:107], v[192:195], v[128:143]\nv_exp_f32 v172, v172\nv_exp_f32 v173, v173\nv_exp_f32 v174, v174\n" \
  "v_exp_f32 v175, v175\ns_nop 0\nv_cvt_pk_bf16_f32 v196, v168, v169\nv_cvt_pk_bf16_f32 v197, v170, v171\nv_cvt_pk_bf16_f32 v198, v172, v173\n" \
  "v_cvt_pk_bf16_f32 v199, v174, v175\ns_nop 1\nv_mfma_f32_32x32x16_bf16 v[128:143], v[48:51], v[196:199], v[128:143]\n.Lfz_comb:\ns_setprio 0\nv_xor_b32 v86, 0x400, v86\n" \
  "v_mov_b32 v9, 155664\ns_waitcnt lgkmcnt(0)\ns_barrier\nds_read_b32 v9, v9\nds_read_b32 v6, v86\nds_read_b128 v[144:147], v13 offset:57344\n" \
  "ds_read_b128 v[148:151], v13 offset:58368\nds_read_b64_tr_b16 v[160:161], v76 offset:8192\nds_read_b64_tr_b16 v[162:163], v76 offset:9216\n" \
  "ds_read_b64_tr_b16 v[164:165], v77 offset:8192\nds_read_b64_tr_b16 v[166:167], v77 offset:9216\nds_read_b64_tr_b16 v[168:169], v78 offset:8192\n" \
  "ds_read_b64_tr_b16 v[170:171], v78 offset:9216\nds_read_b64_tr_b16 v[172:173], v79 offset:8192\nds_read_b64_tr_b16 v[174:175], v79 offset:9216\ns_waitcnt lgkmcnt(10)\n" \
  "v_add_f32 v8, v6, v136\nv_rcp_f32 v6, v8\nv_readfirstlane_b32 s10, v9\ns_waitcnt lgkmcnt(8)\nv_pk_add_f32 v[128:129], v[128:129], v[144:145]\n" \
  "v_pk_add_f32 v[130:131], v[130:131], v[146:147]\nv_pk_add_f32 v[132:133], v[132:133], v[148:149]\nv_pk_add_f32 v[134:135], v[134:135], v[150:151]\n" \
  "v_pk_mul_f32 v[128:129], v[128:129], v[6:7] op_sel_hi:[1,0]\nv_pk_mul_f32 v[130:131], v[130:131], v[6:7] op_sel_hi:[1,0]\n" \
  "v_pk_mul_f32 v[132:133], v[132:133], v[6:7] op_sel_hi:[1,0]\nv_pk_mul_f32 v[134:135], v[134:135], v[6:7] op_sel_hi:[1,0]\nv_cvt_pk_bf16_f32 v120, v128, v129\n" \
  "v_cvt_pk_bf16_f32 v121, v130, v131\nv_cvt_pk_bf16_f32 v122, v132, v133\nv_cvt_pk_bf16_f32 v123, v134, v135\nglobal_store_dwordx4 v11, v[120:123], s[38:39] sc1\n" \
  "v_pk_add_f32 v[6:7], v[128:129], v[130:131]\nv_pk_add_f32 v[144:145], v[132:133], v[134:135]\nv_subrev_u32 v8, 0x2b800000, v8\nv_pk_add_f32 v[6:7], v[6:7], v[144:145]\n" \
  "v_cmp_lt_u32 vcc, 0x28000000, v8\ns_movk_i32 s42, 0x207\nv_add_f32 v6, v6, v7\nv_cmp_class_f32 s[40:41], v6, s42\ns_or_b64 vcc, vcc, s[40:41]\ns_cbranch_vccnz .Lfz_slow\n" \
  ".Lfz_slowr:\nv_add3_u32 v124, v120, v121, v122\nv_add_u32 v124, v124, v123\nds_read_b64_tr_b16 v[176:177], v76 offset:12288\n" \
  "ds_read_b64_tr_b16 v[178:179], v76 offset:13312\nv_add_u32_dpp v124, v124, v124 quad_perm:[1,0,3,2] row_mask:0xf bank_mask:0xf\n" \
  "ds_read_b64_tr_b16 v[180:181], v77 offset:12288\nds_read_b64_tr_b16 v[182:183], v77 offset:13312\n" \
  "v_add_u32_dpp v124, v124, v124 quad_perm:[2,3,0,1] row_mask:0xf bank_mask:0xf\nv_sub_u32 v124, s35, v124\nds_read_b64_tr_b16 v[184:185], v78 offset:12288\n" \
  "ds_read_b64_tr_b16 v[186:187], v78 offset:13312\nv_mov_b32_dpp v125, v124 row_shl:4 row_mask:0xf bank_mask:0xf\n" \
  "v_mov_b32_dpp v126, v124 row_shl:8 row_mask:0xf bank_mask:0xf\nv_mov_b32_dpp v127, v124 row_shl:12 row_mask:0xf bank_mask:0xf\ns_mov_b32 exec_lo, 0x00010001\n" \
  "s_mov_b32 exec_hi, 0x00010001\nglobal_store_dwordx4 v84, v[124:127], s[38:39] sc1\ns_mov_b64 exec, -1\ns_cmp_lt_u32 s28, 4\ns_cbranch_scc1 .Lfz_lt\n.Lfz_br:\n" \
  "s_waitcnt vmcnt(2)\n.Lfz_br2:\ns_andn2_b32 s6, 2047, s10\ns_lshr_b32 s6, s6, 4\ns_lshl_b32 s7, s1, 2\ns_add_u32 s7, s7, s33\ns_mov_b32 s5, 0\n" \
  "v_bitop3_b32 v6, v221, v223, s48 bitop3:0x7e\nv_permlane16_swap_b32 v216, v218\nv_cmp_ne_u32 vcc, 0, v6\nv_permlane16_swap_b32 v217, v219\ns_cbranch_vccnz .Lfz_st\n" \
  ".Lfz_str:\nv_and_b32 v220, 0xffff0000, v220\nv_lshlrev_b32 v32, 16, v216\nds_read_b64_tr_b16 v[188:189], v79 offset:12288\nv_and_b32 v33, 0xffff0000, v216\n" \
  "v_add_f32 v6, v32, v220\nds_read_b64_tr_b16 v[190:191], v79 offset:13312\nv_add_f32 v7, v33, v220\nv_cvt_pk_bf16_f32 v6, v6, v7\n" \
  "ds_read_b64_tr_b16 v[192:193], v76 offset:16384\nv_and_b32 v7, 0xffff0000, v6\nv_lshlrev_b32 v6, 16, v6\nds_read_b64_tr_b16 v[194:195], v76 offset:17408\n" \
  "v_mul_f32 v7, v7, v222\nv_mul_f32 v6, v6, v222\nds_read_b64_tr_b16 v[196:197], v77 offset:16384\nv_cvt_pk_bf16_f32 v6, v6, v7\nv_and_b32 v7, 0xffff0000, v6\n" \
  "ds_read_b64_tr_b16 v[198:199], v77 offset:17408\nv_lshlrev_b32 v6, 16, v6\nv_add_f32 v33, v33, v7\nds_read_b64_tr_b16 v[200:201], v78 offset:16384\n" \
  "v_add_f32 v32, v32, v6\nv_lshlrev_b32 v34, 16, v217\nds_read_b64_tr_b16 v[202:203], v78 offset:17408\nv_and_b32 v35, 0xffff0000, v217\nv_add_f32 v6, v34, v220\n" \
  "ds_read_b64_tr_b16 v[204:205], v79 offset:16384\nv_add_f32 v7, v35, v220\nv_cvt_pk_bf16_f32 v6, v6, v7\nds_read_b64_tr_b16 v[206:207], v79 offset:17408\n" \
  "v_and_b32 v7, 0xffff0000, v6\nv_lshlrev_b32 v6, 16, v6\nds_read_b64_tr_b16 v[128:129], v76 offset:0\nv_mul_f32 v7, v7, v222\nv_mul_f32 v6, v6, v222\n" \
  "ds_read_b64_tr_b16 v[130:131], v76 offset:1024\nv_cvt_pk_bf16_f32 v6, v6, v7\nv_and_b32 v7, 0xffff0000, v6\nds_read_b64_tr_b16 v[132:133], v77 offset:0\n" \
  "v_lshlrev_b32 v6, 16, v6\nv_add_f32 v35, v35, v7\nds_read_b64_tr_b16 v[134:135], v77 offset:1024\nv_add_f32 v34, v34, v6\nv_lshlrev_b32 v36, 16, v218\n" \
  "ds_read_b64_tr_b16 v[136:137], v78 offset:0\nv_and_b32 v37, 0xffff0000, v218\nv_add_f32 v6, v36, v220\nds_read_b64_tr_b16 v[138:139], v78 offset:1024\n" \
  "v_add_f32 v7, v37, v220\nv_cvt_pk_bf16_f32 v6, v6, v7\nds_read_b64_tr_b16 v[140:141], v79 offset:0\nv_and_b32 v7, 0xffff0000, v6\nv_lshlrev_b32 v6, 16, v6\n" \
  "ds_read_b64_tr_b16 v[142:143], v79 offset:1024\nv_mul_f32 v7, v7, v222\nv_mul_f32 v6, v6, v222\nds_read_b64_tr_b16 v[144:145], v76 offset:4096\n" \
  "v_cvt_pk_bf16_f32 v6, v6, v7\nv_and_b32 v7, 0xffff0000, v6\nds_read_b64_tr_b16 v[146:147], v76 offset:5120\nv_lshlrev_b32 v6, 16, v6\nv_add_f32 v37, v37, v7\n" \
  "ds_read_b64_tr_b16 v[148:149], v77 offset:4096\nv_add_f32 v36, v36, v6\nv_lshlrev_b32 v38, 16, v219\nds_read_b64_tr_b16 v[150:151], v77 offset:5120\n" \
  "v_and_b32 v39, 0xffff0000, v219\nv_add_f32 v6, v38, v220\nds_read_b64_tr_b16 v[152:153], v78 offset:4096\nv_add_f32 v7, v39, v220\nv_cvt_pk_bf16_f32 v6, v6, v7\n" \
  "ds_read_b64_tr_b16 v[154:155], v78 offset:5120\nv_and_b32 v7, 0xffff0000, v6\nv_lshlrev_b32 v6, 16, v6\nds_read_b64_tr_b16 v[156:157], v79 offset:4096\n" \
  "v_mul_f32 v7, v7, v222\nv_mul_f32 v6, v6, v222\nds_read_b64_tr_b16 v[158:159], v79 offset:5120\nv_cvt_pk_bf16_f32 v6, v6, v7\nv_and_b32 v7, 0xffff0000, v6\n" \
  "v_lshlrev_b32 v6, 16, v6\nv_add_f32 v39, v39, v7\nv_add_f32 v38, v38, v6\nv_mov_b64 v[40:41], 0\nv_mov_b64 v[42:43], 0\nv_mov_b64 v[44:45], 0\nv_mov_b64 v[46:47], 0\n" \
  "ds_read_b64_tr_b16 v[208:209], v76 offset:20480\nds_read_b64_tr_b16 v[210:211], v76 offset:21504\nds_read_b64_tr_b16 v[212:213], v77 offset:20480\n" \
  "ds_read_b64_tr_b16 v[214:215], v77 offset:21504\nds_read_b64_tr_b16 v[216:217], v78 offset:20480\nds_read_b64_tr_b16 v[218:219], v78 offset:21504\n" \
  "ds_read_b64_tr_b16 v[220:221], v79 offset:20480\nds_read_b64_tr_b16 v[222:223], v79 offset:21504\nds_read_b64_tr_b16 v[224:225], v76 offset:24576\n" \
  "ds_read_b64_tr_b16 v[226:227], v76 offset:25600\nds_read_b64_tr_b16 v[228:229], v77 offset:24576\nds_read_b64_tr_b16 v[230:231], v77 offset:25600\n" \
  "ds_read_b64_tr_b16 v[232:233], v78 offset:24576\nds_read_b64_tr_b16 v[234:235], v78 offset:25600\nds_read_b64_tr_b16 v[236:237], v79 offset:24576\n" \
  "ds_read_b64_tr_b16 v[238:239], v79 offset:25600\nds_read_b64_tr_b16 v[240:241], v76 offset:28672\nds_read_b64_tr_b16 v[242:243], v76 offset:29696\n" \
  "ds_read_b64_tr_b16 v[244:245], v77 offset:28672\nds_read_b64_tr_b16 v[246:247], v77 offset:29696\nds_read_b64_tr_b16 v[248:249], v78 offset:28672\n" \
  "ds_read_b64_tr_b16 v[250:251], v78 offset:29696\nds_read_b64_tr_b16 v[252:253], v79 offset:28672\nds_read_b64_tr_b16 v[254:255], v79 offset:29696\ns_cmp_lg_u32 s11, 0\n" \
  "s_cbranch_scc1 .Lfz_o2\nglobal_load_dwordx4 v[88:91], v2, s[60:61] sc1\nglobal_load_dwordx4 v[92:95], v2, s[60:61] offset:1024 sc1\ns_mov_b64 exec, s[50:51]\n" \
  "global_load_dword v72, v85, s[60:61] sc1\ns_mov_b64 exec, -1\nglobal_load_dwordx4 v[96:99], v2, s[62:63] sc1\n" \
  "global_load_dwordx4 v[100:103], v2, s[62:63] offset:1024 sc1\ns_mov_b64 exec, s[50:51]\nglobal_load_dword v73, v85, s[62:63] sc1\ns_mov_b64 exec, -1\n.Lfz_o2:\n" \
  "global_load_dwordx4 v[104:107], v2, s[64:65] sc1\nglobal_load_dwordx4 v[108:111], v2, s[64:65] offset:1024 sc1\ns_mov_b64 exec, s[50:51]\n" \
  "global_load_dword v74, v85, s[64:65] sc1\ns_mov_b64 exec, -1\n.Lfz_o3:\ns_waitcnt lgkmcnt(0)\ns_waitcnt vmcnt(6)\nglobal_load_dwordx4 v[112:115], v2, s[66:67] sc1\n" \
  "global_load_dwordx4 v[116:119], v2, s[66:67] offset:1024 sc1\ns_mov_b64 exec, s[50:51]\nglobal_load_dword v75, v85, s[66:67] sc1\ns_mov_b64 exec, -1\n" \
  "v_add3_u32 v6, v88, v89, v90\nv_add3_u32 v7, v91, v92, v93\nv_add3_u32 v8, v94, v95, v72\nv_add3_u32 v6, v6, v7, v8\ns_nop 1\n" \
  "v_add_u32_dpp v6, v6, v6 quad_perm:[1,0,3,2] row_mask:0xf bank_mask:0xf\ns_nop 1\nv_add_u32_dpp v6, v6, v6 quad_perm:[2,3,0,1] row_mask:0xf bank_mask:0xf\n" \
  "v_cmp_ne_u32 vcc, s36, v6\ns_cbranch_vccnz .Lfz_ort0\nv_mfma_f32_16x16x32_bf16 v[136:139], v[136:139], v[88:91], 0\n" \
  "v_mfma_f32_16x16x32_bf16 v[140:143], v[140:143], v[88:91], 0\nv_mfma_f32_16x16x32_bf16 v[136:139], v[152:155], v[92:95], v[136:139]\n" \
  "v_mfma_f32_16x16x32_bf16 v[140:143], v[156:159], v[92:95], v[140:143]\nv_mfma_f32_16x16x32_bf16 v[128:131], v[128:131], v[88:91], 0\n" \
  "v_mfma_f32_16x16x32_bf16 v[132:135], v[132:135], v[88:91], 0\nv_mfma_f32_16x16x32_bf16 v[128:131], v[144:147], v[92:95], v[128:131]\n" \
  "v_mfma_f32_16x16x32_bf16 v[132:135], v[148:151], v[92:95], v[132:135]\n.Lfz_ook0:\ns_waitcnt vmcnt(6)\nv_add3_u32 v6, v96, v97, v98\nv_add3_u32 v7, v99, v100, v101\n" \
  "v_add3_u32 v8, v102, v103, v73\nv_add3_u32 v6, v6, v7, v8\ns_nop 1\nv_add_u32_dpp v6, v6, v6 quad_perm:[1,0,3,2] row_mask:0xf bank_mask:0xf\ns_nop 1\n" \
  "v_add_u32_dpp v6, v6, v6 quad_perm:[2,3,0,1] row_mask:0xf bank_mask:0xf\nv_cmp_ne_u32 vcc, s36, v6\ns_cmp_lg_u32 s5, 0\ns_cbranch_scc1 .Lfz_oa1\n" \
  "v_pk_add_f32 v[40:41], v[40:41], v[136:137]\nv_pk_add_f32 v[42:43], v[42:43], v[138:139]\nv_pk_add_f32 v[44:45], v[44:45], v[140:141]\n" \
  "v_pk_add_f32 v[46:47], v[46:47], v[142:143]\nv_pk_add_f32 v[32:33], v[32:33], v[128:129]\nv_pk_add_f32 v[34:35], v[34:35], v[130:131]\n" \
  "v_pk_add_f32 v[36:37], v[36:37], v[132:133]\nv_pk_add_f32 v[38:39], v[38:39], v[134:135]\n.Lfz_oa1:\ns_cbranch_vccnz .Lfz_ort1\n" \
  "v_mfma_f32_16x16x32_bf16 v[168:171], v[168:171], v[96:99], 0\nv_mfma_f32_16x16x32_bf16 v[172:175], v[172:175], v[96:99], 0\n" \
  "v_mfma_f32_16x16x32_bf16 v[168:171], v[184:187], v[100:103], v[168:171]\nv_mfma_f32_16x16x32_bf16 v[172:175], v[188:191], v[100:103], v[172:175]\n" \
  "v_mfma_f32_16x16x32_bf16 v[160:163], v[160:163], v[96:99], 0\nv_mfma_f32_16x16x32_bf16 v[164:167], v[164:167], v[96:99], 0\n" \
  "v_mfma_f32_16x16x32_bf16 v[160:163], v[176:179], v[100:103], v[160:163]\nv_mfma_f32_16x16x32_bf16 v[164:167], v[180:183], v[100:103], v[164:167]\n.Lfz_ook1:\n" \
  "s_waitcnt vmcnt(3)\ns_cmp_eq_u32 s6, s7\ns_cbranch_scc1 .Lfz_y0\n.Lfz_y0r:\nv_add3_u32 v6, v104, v105, v106\nv_add3_u32 v7, v107, v108, v109\n" \
  "v_add3_u32 v8, v110, v111, v74\nv_add3_u32 v6, v6, v7, v8\ns_nop 1\nv_add_u32_dpp v6, v6, v6 quad_perm:[1,0,3,2] row_mask:0xf bank_mask:0xf\ns_nop 1\n" \
  "v_add_u32_dpp v6, v6, v6 quad_perm:[2,3,0,1] row_mask:0xf bank_mask:0xf\nv_cmp_ne_u32 vcc, s36, v6\ns_cmp_lg_u32 s5, 0\ns_cbranch_scc1 .Lfz_oa2\n" \
  "v_pk_add_f32 v[40:41], v[40:41], v[168:169]\nv_pk_add_f32 v[42:43], v[42:43], v[170:171]\nv_pk_add_f32 v[44:45], v[44:45], v[172:173]\n" \
  "v_pk_add_f32 v[46:47], v[46:47], v[174:175]\nv_pk_add_f32 v[32:33], v[32:33], v[160:161]\nv_pk_add_f32 v[34:35], v[34:35], v[162:163]\n" \
  "v_pk_add_f32 v[36:37], v[36:37], v[164:165]\nv_pk_add_f32 v[38:39], v[38:39], v[166:167]\n.Lfz_oa2:\ns_cbranch_vccnz .Lfz_ort2\n" \
  "v_mfma_f32_16x16x32_bf16 v[200:203], v[200:203], v[104:107], 0\nv_mfma_f32_16x16x32_bf16 v[204:207], v[204:207], v[104:107], 0\n" \
  "v_mfma_f32_16x16x32_bf16 v[200:203], v[216:219], v[108:111], v[200:203]\nv_mfma_f32_16x16x32_bf16 v[204:207], v[220:223], v[108:111], v[204:207]\n" \
  "v_mfma_f32_16x16x32_bf16 v[192:195], v[192:195], v[104:107], 0\nv_mfma_f32_16x16x32_bf16 v[196:199], v[196:199], v[104:107], 0\n" \
  "v_mfma_f32_16x16x32_bf16 v[192:195], v[208:211], v[108:111], v[192:195]\nv_mfma_f32_16x16x32_bf16 v[196:199], v[212:215], v[108:111], v[196:199]\n.Lfz_ook2:\n" \
  "s_waitcnt vmcnt(0)\nv_add3_u32 v6, v112, v113, v114\nv_add3_u32 v7, v115, v116, v117\nv_add3_u32 v8, v118, v119, v75\nv_add3_u32 v6, v6, v7, v8\ns_nop 1\n" \
  "v_add_u32_dpp v6, v6, v6 quad_perm:[1,0,3,2] row_mask:0xf bank_mask:0xf\ns_nop 1\nv_add_u32_dpp v6, v6, v6 quad_perm:[2,3,0,1] row_mask:0xf bank_mask:0xf\n" \
  "v_cmp_ne_u32 vcc, s36, v6\ns_cmp_lg_u32 s5, 0\ns_cbranch_scc1 .Lfz_oa3\nv_pk_add_f32 v[40:41], v[40:41], v[200:201]\nv_pk_add_f32 v[42:43], v[42:43], v[202:203]\n" \
  "v_pk_add_f32 v[44:45], v[44:45], v[204:205]\nv_pk_add_f32 v[46:47], v[46:47], v[206:207]\nv_pk_add_f32 v[32:33], v[32:33], v[192:193]\n" \
  "v_pk_add_f32 v[34:35], v[34:35], v[194:195]\nv_pk_add_f32 v[36:37], v[36:37], v[196:197]\nv_pk_add_f32 v[38:39], v[38:39], v[198:199]\n.Lfz_oa3:\n" \
  "s_cbranch_vccnz .Lfz_ort3\nv_mfma_f32_16x16x32_bf16 v[232:235], v[232:235], v[112:115], 0\nv_mfma_f32_16x16x32_bf16 v[236:239], v[236:239], v[112:115], 0\n" \
  "v_mfma_f32_16x16x32_bf16 v[232:235], v[248:251], v[116:119], v[232:235]\nv_mfma_f32_16x16x32_bf16 v[236:239], v[252:255], v[116:119], v[236:239]\n" \
  "v_mfma_f32_16x16x32_bf16 v[224:227], v[224:227], v[112:115], 0\nv_mfma_f32_16x16x32_bf16 v[228:231], v[228:231], v[112:115], 0\n" \
  "v_mfma_f32_16x16x32_bf16 v[224:227], v[240:243], v[116:119], v[224:227]\nv_mfma_f32_16x16x32_bf16 v[228:231], v[244:247], v[116:119], v[228:231]\n.Lfz_ook3:\n" \
  "s_cmp_lg_u32 s5, 0\ns_cbranch_scc1 .Lfz_op\n.Lfz_opr:\ns_nop 0\nv_pk_add_f32 v[40:41], v[40:41], v[232:233]\nv_pk_add_f32 v[42:43], v[42:43], v[234:235]\n" \
  "ds_write_b128 v14, v[40:43] offset:0\nv_pk_add_f32 v[44:45], v[44:45], v[236:237]\nv_pk_add_f32 v[46:47], v[46:47], v[238:239]\nds_write_b128 v14, v[44:47] offset:1024\n" \
  "v_pk_add_f32 v[32:33], v[32:33], v[224:225]\nv_pk_add_f32 v[34:35], v[34:35], v[226:227]\nv_pk_add_f32 v[36:37], v[36:37], v[228:229]\n" \
  "v_pk_add_f32 v[38:39], v[38:39], v[230:231]\ns_waitcnt lgkmcnt(0)\ns_barrier\nds_read_b128 v[40:43], v13 offset:0\nds_read_b128 v[44:47], v13 offset:1024\n" \
  "s_cmp_eq_u32 s6, s7\ns_cbranch_scc1 .Lfz_y\n.Lfz_yr:\ns_waitcnt lgkmcnt(1)\nv_pk_add_f32 v[32:33], v[32:33], v[40:41]\nv_pk_add_f32 v[34:35], v[34:35], v[42:43]\n" \
  "s_waitcnt lgkmcnt(0)\nv_pk_add_f32 v[36:37], v[36:37], v[44:45]\nv_pk_add_f32 v[38:39], v[38:39], v[46:47]\nv_cvt_pk_bf16_f32 v48, v32, v33\n" \
  "v_cvt_pk_bf16_f32 v50, v36, v37\nv_cvt_pk_bf16_f32 v49, v34, v35\nv_cvt_pk_bf16_f32 v51, v38, v39\nv_permlane16_swap_b32 v48, v50\ns_nop 0\n" \
  "v_permlane16_swap_b32 v49, v51\nglobal_store_dwordx4 v12, v[48:51], s[22:23] sc1\ns_or_b32 s40, s26, s27\ns_cbranch_scc1 .Lfz_end\nv_mov_b32 v6, 0\nv_mov_b32 v8, s48\n" \
  "v_mov_b32 v9, s49\ns_mov_b64 exec, 1\nglobal_store_dwordx2 v6, v[8:9], s[24:25] sc1\ns_mov_b64 exec, -1\n.Lfz_end:\ns_endpgm\n.Lfz_dm1:\ns_mov_b64 exec, 1\n" \
  "global_load_dword v71, v2, s[8:9]\nglobal_load_dword v71, v2, s[8:9]\nglobal_load_dword v71, v2, s[8:9]\ns_branch .Lfz_dr1\n.Lfz_dm2:\ns_mov_b64 exec, 1\n" \
  "global_load_dword v70, v2, s[8:9]\nglobal_load_dword v70, v2, s[8:9]\nglobal_load_dword v70, v2, s[8:9]\ns_branch .Lfz_dr2\n.Lfz_dm3:\ns_mov_b64 exec, 1\n" \
  "global_load_dword v69, v2, s[8:9]\nglobal_load_dword v69, v2, s[8:9]\nglobal_load_dword v69, v2, s[8:9]\ns_branch .Lfz_dr3\n.Lfz_dm4:\ns_mov_b64 exec, 1\n" \
  "global_load_dword v68, v2, s[8:9]\nglobal_load_dword v68, v2, s[8:9]\nglobal_load_dword v68, v2, s[8:9]\ns_branch .Lfz_dr4\n.Lfz_ad:\ns_mov_b64 exec, 1\n" \
  "global_load_dword v248, v2, s[8:9]\nglobal_load_dword v248, v2, s[8:9]\nglobal_load_dword v248, v2, s[8:9]\nglobal_load_dword v248, v2, s[8:9]\ns_mov_b64 exec, -1\n" \
  "s_branch .Lfz_adr\n.Lfz_dm5:\ns_mov_b64 exec, 1\nglobal_load_dword v71, v2, s[8:9]\nglobal_load_dword v71, v2, s[8:9]\nglobal_load_dword v71, v2, s[8:9]\n" \
  "s_branch .Lfz_dr5\n.Lfz_dm6:\ns_mov_b64 exec, 1\nglobal_load_dword v70, v2, s[8:9]\nglobal_load_dword v70, v2, s[8:9]\nglobal_load_dword v70, v2, s[8:9]\n" \
  "s_branch .Lfz_dr6\n.Lfz_dm7:\ns_mov_b64 exec, 1\nglobal_load_dword v69, v2, s[8:9]\nglobal_load_dword v69, v2, s[8:9]\nglobal_load_dword v69, v2, s[8:9]\n" \
  "s_branch .Lfz_dr7\n.Lfz_dm8:\ns_mov_b64 exec, 1\nglobal_load_dword v68, v2, s[8:9]\nglobal_load_dword v68, v2, s[8:9]\nglobal_load_dword v68, v2, s[8:9]\n" \
  "s_branch .Lfz_dr8\n.Lfz_wdb0:\ns_lshl_b32 s3, s27, 10\ns_barrier\ns_mul_i32 s40, s34, 16384\ns_add_u32 s40, s40, 73728\ns_mul_i32 s41, s34, 16384\ns_add_u32 s41, s41, 0\n" \
  "s_lshl_b32 s42, s33, 10\ns_add_u32 m0, s40, s42\ns_add_u32 s41, s41, s42\ns_add_u32 s42, s20, s41\ns_addc_u32 s43, s21, 0\nglobal_load_lds_dwordx4 v87, s[42:43]\n" \
  "s_mul_i32 s40, s34, 16384\ns_add_u32 s40, s40, 77824\ns_mul_i32 s41, s34, 16384\ns_add_u32 s41, s41, 4096\ns_lshl_b32 s42, s33, 10\ns_add_u32 m0, s40, s42\n" \
  "s_add_u32 s41, s41, s42\ns_add_u32 s42, s20, s41\ns_addc_u32 s43, s21, 0\nglobal_load_lds_dwordx4 v87, s[42:43]\ns_mul_i32 s40, s34, 16384\ns_add_u32 s40, s40, 81920\n" \
  "s_mul_i32 s41, s34, 16384\ns_add_u32 s41, s41, 8192\ns_lshl_b32 s42, s33, 10\ns_add_u32 m0, s40, s42\ns_add_u32 s41, s41, s42\ns_add_u32 s42, s20, s41\n" \
  "s_addc_u32 s43, s21, 0\nglobal_load_lds_dwordx4 v87, s[42:43]\ns_mul_i32 s40, s34, 16384\ns_add_u32 s40, s40, 86016\ns_mul_i32 s41, s34, 16384\n" \
  "s_add_u32 s41, s41, 12288\ns_lshl_b32 s42, s33, 10\ns_add_u32 m0, s40, s42\ns_add_u32 s41, s41, s42\ns_add_u32 s42, s20, s41\ns_addc_u32 s43, s21, 0\n" \
  "global_load_lds_dwordx4 v87, s[42:43]\ns_cmp_lt_u32 s11, 2\ns_cbranch_scc1 .Lfz_lp1\ns_mul_i32 s40, s34, 16384\ns_add_u32 s40, s40, 106496\ns_mul_i32 s41, s34, 16384\n" \
  "s_add_u32 s41, s41, 32768\ns_lshl_b32 s42, s33, 10\ns_add_u32 m0, s40, s42\ns_add_u32 s41, s41, s42\ns_add_u32 s42, s20, s41\ns_addc_u32 s43, s21, 0\n" \
  "global_load_lds_dwordx4 v87, s[42:43]\ns_mul_i32 s40, s34, 16384\ns_add_u32 s40, s40, 110592\ns_mul_i32 s41, s34, 16384\ns_add_u32 s41, s41, 36864\n" \
  "s_lshl_b32 s42, s33, 10\ns_add_u32 m0, s40, s42\ns_add_u32 s41, s41, s42\ns_add_u32 s42, s20, s41\ns_addc_u32 s43, s21, 0\nglobal_load_lds_dwordx4 v87, s[42:43]\n" \
  "s_mul_i32 s40, s34, 16384\ns_add_u32 s40, s40, 114688\ns_mul_i32 s41, s34, 16384\ns_add_u32 s41, s41, 40960\ns_lshl_b32 s42, s33, 10\ns_add_u32 m0, s40, s42\n" \
  "s_add_u32 s41, s41, s42\ns_add_u32 s42, s20, s41\ns_addc_u32 s43, s21, 0\nglobal_load_lds_dwordx4 v87, s[42:43]\ns_mul_i32 s40, s34, 16384\ns_add_u32 s40, s40, 118784\n" \
  "s_mul_i32 s41, s34, 16384\ns_add_u32 s41, s41, 45056\ns_lshl_b32 s42, s33, 10\ns_add_u32 m0, s40, s42\ns_add_u32 s41, s41, s42\ns_add_u32 s42, s20, s41\n" \
  "s_addc_u32 s43, s21, 0\nglobal_load_lds_dwordx4 v87, s[42:43]\n.Lfz_lp1:\ns_branch .Lfz_wdrb0\n.Lfz_rb0:\ns_mov_b32 s37, 262144\n.Lfz_lb0:\n" \
  "global_load_dwordx4 v[100:103], v2, s[58:59] sc1\nglobal_load_dwordx4 v[116:119], v2, s[58:59] offset:1024 sc1\ns_mov_b64 exec, s[50:51]\n" \
  "global_load_dword v71, v3, s[58:59] sc1\ns_mov_b64 exec, -1\ns_waitcnt vmcnt(0)\nv_add3_u32 v6, v100, v101, v102\nv_add3_u32 v7, v103, v116, v117\n" \
  "v_add3_u32 v8, v118, v119, v71\nv_add3_u32 v6, v6, v7, v8\ns_nop 1\nv_add_u32_dpp v6, v6, v6 quad_perm:[1,0,3,2] row_mask:0xf bank_mask:0xf\ns_nop 1\n" \
  "v_add_u32_dpp v6, v6, v6 quad_perm:[2,3,0,1] row_mask:0xf bank_mask:0xf\nv_cmp_ne_u32 vcc, s36, v6\ns_cbranch_vccz .Lfz_mb0\ns_sub_u32 s37, s37, 1\n" \
  "s_cbranch_scc0 .Lfz_lb0\n.Lfz_mb0:\nv_mfma_f32_32x32x16_bf16 v[176:191], v[100:103], v[212:215], 0\ns_nop 6\ns_branch .Lfz_kb0\n.Lfz_xb0:\ns_nop 0\n" \
  "v_mfma_f32_32x32x16_bf16 v[128:143], v[64:67], v[196:199], v[128:143]\ns_waitcnt vmcnt(0)\ns_branch .Lfz_xg\n.Lfz_wdb2:\ns_add_u32 s40, s31, 7\ns_and_b32 s40, s40, 7\n" \
  "s_lshl_b32 s40, s40, 16\ns_add_u32 m0, s3, 57344\ns_add_u32 s40, s12, s40\ns_addc_u32 s41, s13, 0\nglobal_load_lds_dwordx4 v10, s[40:41]\ns_add_u32 s40, s31, 6\n" \
  "s_and_b32 s40, s40, 7\ns_lshl_b32 s40, s40, 16\ns_add_u32 m0, s3, 49152\ns_add_u32 s40, s12, s40\ns_addc_u32 s41, s13, 0\nglobal_load_lds_dwordx4 v10, s[40:41]\n" \
  "s_add_u32 s40, s31, 5\ns_and_b32 s40, s40, 7\ns_lshl_b32 s40, s40, 16\ns_add_u32 m0, s3, 40960\ns_add_u32 s40, s12, s40\ns_addc_u32 s41, s13, 0\n" \
  "global_load_lds_dwordx4 v10, s[40:41]\ns_branch .Lfz_wdrb2\n.Lfz_rb2:\ns_mov_b32 s37, 262144\n.Lfz_lb2:\nglobal_load_dwordx4 v[96:99], v2, s[56:57] sc1\n" \
  "global_load_dwordx4 v[112:115], v2, s[56:57] offset:1024 sc1\ns_mov_b64 exec, s[50:51]\nglobal_load_dword v70, v3, s[56:57] sc1\ns_mov_b64 exec, -1\ns_waitcnt vmcnt(0)\n" \
  "v_add3_u32 v6, v96, v97, v98\nv_add3_u32 v7, v99, v112, v113\nv_add3_u32 v8, v114, v115, v70\nv_add3_u32 v6, v6, v7, v8\ns_nop 1\n" \
  "v_add_u32_dpp v6, v6, v6 quad_perm:[1,0,3,2] row_mask:0xf bank_mask:0xf\ns_nop 1\nv_add_u32_dpp v6, v6, v6 quad_perm:[2,3,0,1] row_mask:0xf bank_mask:0xf\n" \
  "v_cmp_ne_u32 vcc, s36, v6\ns_cbranch_vccz .Lfz_mb2\ns_sub_u32 s37, s37, 1\ns_cbranch_scc0 .Lfz_lb2\n.Lfz_mb2:\n" \
  "v_mfma_f32_32x32x16_bf16 v[176:191], v[96:99], v[212:215], 0\ns_nop 6\ns_branch .Lfz_kb2\n.Lfz_xb2:\ns_nop 0\n" \
  "v_mfma_f32_32x32x16_bf16 v[128:143], v[60:63], v[196:199], v[128:143]\ns_waitcnt vmcnt(4)\ns_branch .Lfz_xg\n.Lfz_gs:\ns_mov_b32 s37, 262144\n.Lfz_gls:\n" \
  "s_add_u32 s46, s24, 4096\ns_addc_u32 s47, s25, 0\nglobal_load_dwordx4 v[224:227], v2, s[46:47] sc1\nglobal_load_dwordx4 v[228:231], v2, s[46:47] offset:1024 sc1\n" \
  "s_waitcnt vmcnt(0)\nv_bitop3_b32 v6, v225, v227, s48 bitop3:0x7e\nv_bitop3_b32 v7, v229, v231, s48 bitop3:0x7e\nv_max3_u32 v8, v224, v226, v228\nv_or_b32 v6, v6, v7\n" \
  "v_max_u32 v8, v8, v230\nv_cmp_ne_u32 vcc, 0, v6\ns_cbranch_vccz .Lfz_gsr\ns_sub_u32 s37, s37, 1\ns_cbranch_scc0 .Lfz_gls\ns_branch .Lfz_gsr\n.Lfz_wdb4:\n" \
  "s_add_u32 s40, s31, 4\ns_and_b32 s40, s40, 7\ns_lshl_b32 s40, s40, 16\ns_add_u32 m0, s3, 32768\ns_add_u32 s40, s12, s40\ns_addc_u32 s41, s13, 0\n" \
  "global_load_lds_dwordx4 v10, s[40:41]\ns_add_u32 s40, s31, 3\ns_and_b32 s40, s40, 7\ns_lshl_b32 s40, s40, 16\ns_add_u32 m0, s3, 24576\ns_add_u32 s40, s12, s40\n" \
  "s_addc_u32 s41, s13, 0\nglobal_load_lds_dwordx4 v10, s[40:41]\ns_add_u32 s40, s31, 2\ns_and_b32 s40, s40, 7\ns_lshl_b32 s40, s40, 16\ns_add_u32 m0, s3, 16384\n" \
  "s_add_u32 s40, s12, s40\ns_addc_u32 s41, s13, 0\nglobal_load_lds_dwordx4 v10, s[40:41]\ns_branch .Lfz_wdrb4\n.Lfz_rb4:\ns_mov_b32 s37, 262144\n.Lfz_lb4:\n" \
  "global_load_dwordx4 v[92:95], v2, s[54:55] sc1\nglobal_load_dwordx4 v[108:111], v2, s[54:55] offset:1024 sc1\ns_mov_b64 exec, s[50:51]\n" \
  "global_load_dword v69, v3, s[54:55] sc1\ns_mov_b64 exec, -1\ns_waitcnt vmcnt(0)\nv_add3_u32 v6, v92, v93, v94\nv_add3_u32 v7, v95, v108, v109\n" \
  "v_add3_u32 v8, v110, v111, v69\nv_add3_u32 v6, v6, v7, v8\ns_nop 1\nv_add_u32_dpp v6, v6, v6 quad_perm:[1,0,3,2] row_mask:0xf bank_mask:0xf\ns_nop 1\n" \
  "v_add_u32_dpp v6, v6, v6 quad_perm:[2,3,0,1] row_mask:0xf bank_mask:0xf\nv_cmp_ne_u32 vcc, s36, v6\ns_cbranch_vccz .Lfz_mb4\ns_sub_u32 s37, s37, 1\n" \
  "s_cbranch_scc0 .Lfz_lb4\n.Lfz_mb4:\nv_mfma_f32_32x32x16_bf16 v[176:191], v[92:95], v[212:215], 0\ns_nop 6\ns_branch .Lfz_kb4\n.Lfz_xb4:\ns_nop 0\n" \
  "v_mfma_f32_32x32x16_bf16 v[128:143], v[56:59], v[196:199], v[128:143]\ns_branch .Lfz_xw1\n.Lfz_rb6:\ns_mov_b32 s37, 262144\n.Lfz_lb6:\n" \
  "global_load_dwordx4 v[88:91], v2, s[52:53] sc1\nglobal_load_dwordx4 v[104:107], v2, s[52:53] offset:1024 sc1\ns_mov_b64 exec, s[50:51]\n" \
  "global_load_dword v68, v3, s[52:53] sc1\ns_mov_b64 exec, -1\ns_waitcnt vmcnt(0)\nv_add3_u32 v6, v88, v89, v90\nv_add3_u32 v7, v91, v104, v105\n" \
  "v_add3_u32 v8, v106, v107, v68\nv_add3_u32 v6, v6, v7, v8\ns_nop 1\nv_add_u32_dpp v6, v6, v6 quad_perm:[1,0,3,2] row_mask:0xf bank_mask:0xf\ns_nop 1\n" \
  "v_add_u32_dpp v6, v6, v6 quad_perm:[2,3,0,1] row_mask:0xf bank_mask:0xf\nv_cmp_ne_u32 vcc, s36, v6\ns_cbranch_vccz .Lfz_mb6\ns_sub_u32 s37, s37, 1\n" \
  "s_cbranch_scc0 .Lfz_lb6\n.Lfz_mb6:\nv_mfma_f32_32x32x16_bf16 v[176:191], v[88:91], v[212:215], 0\ns_nop 6\ns_branch .Lfz_kb6\n.Lfz_xb6:\ns_nop 0\n" \
  "v_mfma_f32_32x32x16_bf16 v[128:143], v[52:55], v[196:199], v[128:143]\ns_branch .Lfz_xw1\n.Lfz_xg:\nv_bitop3_b32 v6, v225, v227, s48 bitop3:0x7e\n" \
  "v_bitop3_b32 v7, v229, v231, s48 bitop3:0x7e\nv_max3_u32 v8, v224, v226, v228\nv_or_b32 v6, v6, v7\nv_max_u32 v8, v8, v230\nv_cmp_ne_u32 vcc, 0, v6\n" \
  "s_cbranch_vccz .Lfz_gxr\n.Lfz_gx:\ns_mov_b32 s37, 262144\n.Lfz_glx:\ns_add_u32 s46, s24, 4096\ns_addc_u32 s47, s25, 0\nglobal_load_dwordx4 v[224:227], v2, s[46:47] sc1\n" \
  "global_load_dwordx4 v[228:231], v2, s[46:47] offset:1024 sc1\ns_waitcnt vmcnt(0)\nv_bitop3_b32 v6, v225, v227, s48 bitop3:0x7e\n" \
  "v_bitop3_b32 v7, v229, v231, s48 bitop3:0x7e\nv_max3_u32 v8, v224, v226, v228\nv_or_b32 v6, v6, v7\nv_max_u32 v8, v8, v230\nv_cmp_ne_u32 vcc, 0, v6\n" \
  "s_cbranch_vccz .Lfz_gxr\ns_sub_u32 s37, s37, 1\ns_cbranch_scc0 .Lfz_glx\n.Lfz_gxr:\ns_nop 1\nv_max_u32_dpp v8, v8, v8 quad_perm:[1,0,3,2] row_mask:0xf bank_mask:0xf\n" \
  "s_nop 1\nv_max_u32_dpp v8, v8, v8 quad_perm:[2,3,0,1] row_mask:0xf bank_mask:0xf\ns_nop 1\nv_max_u32_dpp v8, v8, v8 row_half_mirror row_mask:0xf bank_mask:0xf\ns_nop 1\n" \
  "v_max_u32_dpp v8, v8, v8 row_mirror row_mask:0xf bank_mask:0xf\ns_nop 0\nv_readlane_b32 s40, v8, 0\nv_readlane_b32 s41, v8, 16\nv_readlane_b32 s42, v8, 32\n" \
  "v_readlane_b32 s43, v8, 48\ns_max_u32 s40, s40, s41\ns_max_u32 s42, s42, s43\ns_max_u32 s10, s40, s42\n.Lfz_xw1:\ns_lshl_b32 s40, s1, 16\ns_lshl_b32 s41, s2, 7\n" \
  "s_add_u32 s40, s40, s41\ns_add_u32 s40, s8, s40\ns_addc_u32 s41, s9, 0\nglobal_load_dwordx4 v[216:219], v12, s[40:41]\nglobal_load_dwordx4 v[220:223], v15, s[60:61] sc1\n" \
  "s_nop 5\nds_write_b128 v14, v[144:147] offset:57344\nds_write_b128 v14, v[148:151] offset:58368\nds_write_b32 v86, v152\ns_branch .Lfz_ae2\n.Lfz_xw0:\n" \
  "s_lshl_b32 s40, s1, 16\ns_lshl_b32 s41, s2, 7\ns_add_u32 s40, s40, s41\ns_add_u32 s40, s8, s40\ns_addc_u32 s41, s9, 0\nglobal_load_dwordx4 v[216:219], v12, s[40:41]\n" \
  "global_load_dwordx4 v[220:223], v15, s[60:61] sc1\ns_nop 5\nds_write_b128 v14, v[144:147] offset:57344\nds_write_b128 v14, v[148:151] offset:58368\n" \
  "ds_write_b32 v86, v152\ns_branch .Lfz_comb\n.Lfz_wda0:\ns_lshl_b32 s3, s27, 10\ns_barrier\ns_mul_i32 s40, s34, 16384\ns_add_u32 s40, s40, 73728\n" \
  "s_mul_i32 s41, s34, 16384\ns_add_u32 s41, s41, 0\ns_lshl_b32 s42, s33, 10\ns_add_u32 m0, s40, s42\ns_add_u32 s41, s41, s42\ns_add_u32 s42, s20, s41\n" \
  "s_addc_u32 s43, s21, 0\nglobal_load_lds_dwordx4 v87, s[42:43]\ns_mul_i32 s40, s34, 16384\ns_add_u32 s40, s40, 77824\ns_mul_i32 s41, s34, 16384\ns_add_u32 s41, s41, 4096\n" \
  "s_lshl_b32 s42, s33, 10\ns_add_u32 m0, s40, s42\ns_add_u32 s41, s41, s42\ns_add_u32 s42, s20, s41\ns_addc_u32 s43, s21, 0\nglobal_load_lds_dwordx4 v87, s[42:43]\n" \
  "s_mul_i32 s40, s34, 16384\ns_add_u32 s40, s40, 81920\ns_mul_i32 s41, s34, 16384\ns_add_u32 s41, s41, 8192\ns_lshl_b32 s42, s33, 10\ns_add_u32 m0, s40, s42\n" \
  "s_add_u32 s41, s41, s42\ns_add_u32 s42, s20, s41\ns_addc_u32 s43, s21, 0\nglobal_load_lds_dwordx4 v87, s[42:43]\ns_mul_i32 s40, s34, 16384\ns_add_u32 s40, s40, 86016\n" \
  "s_mul_i32 s41, s34, 16384\ns_add_u32 s41, s41, 12288\ns_lshl_b32 s42, s33, 10\ns_add_u32 m0, s40, s42\ns_add_u32 s41, s41, s42\ns_add_u32 s42, s20, s41\n" \
  "s_addc_u32 s43, s21, 0\nglobal_load_lds_dwordx4 v87, s[42:43]\ns_cmp_lt_u32 s11, 2\ns_cbranch_scc1 .Lfz_lp2\ns_mul_i32 s40, s34, 16384\ns_add_u32 s40, s40, 106496\n" \
  "s_mul_i32 s41, s34, 16384\ns_add_u32 s41, s41, 32768\ns_lshl_b32 s42, s33, 10\ns_add_u32 m0, s40, s42\ns_add_u32 s41, s41, s42\ns_add_u32 s42, s20, s41\n" \
  "s_addc_u32 s43, s21, 0\nglobal_load_lds_dwordx4 v87, s[42:43]\ns_mul_i32 s40, s34, 16384\ns_add_u32 s40, s40, 110592\ns_mul_i32 s41, s34, 16384\n" \
  "s_add_u32 s41, s41, 36864\ns_lshl_b32 s42, s33, 10\ns_add_u32 m0, s40, s42\ns_add_u32 s41, s41, s42\ns_add_u32 s42, s20, s41\ns_addc_u32 s43, s21, 0\n" \
  "global_load_lds_dwordx4 v87, s[42:43]\ns_mul_i32 s40, s34, 16384\ns_add_u32 s40, s40, 114688\ns_mul_i32 s41, s34, 16384\ns_add_u32 s41, s41, 40960\n" \
  "s_lshl_b32 s42, s33, 10\ns_add_u32 m0, s40, s42\ns_add_u32 s41, s41, s42\ns_add_u32 s42, s20, s41\ns_addc_u32 s43, s21, 0\nglobal_load_lds_dwordx4 v87, s[42:43]\n" \
  "s_mul_i32 s40, s34, 16384\ns_add_u32 s40, s40, 118784\ns_mul_i32 s41, s34, 16384\ns_add_u32 s41, s41, 45056\ns_lshl_b32 s42, s33, 10\ns_add_u32 m0, s40, s42\n" \
  "s_add_u32 s41, s41, s42\ns_add_u32 s42, s20, s41\ns_addc_u32 s43, s21, 0\nglobal_load_lds_dwordx4 v87, s[42:43]\n.Lfz_lp2:\ns_branch .Lfz_wdra0\n.Lfz_ra1:\n" \
  "s_mov_b32 s37, 262144\n.Lfz_la1:\nglobal_load_dwordx4 v[100:103], v2, s[58:59] sc1\nglobal_load_dwordx4 v[116:119], v2, s[58:59] offset:1024 sc1\n" \
  "s_mov_b64 exec, s[50:51]\nglobal_load_dword v71, v3, s[58:59] sc1\ns_mov_b64 exec, -1\ns_waitcnt vmcnt(0)\nv_add3_u32 v6, v100, v101, v102\n" \
  "v_add3_u32 v7, v103, v116, v117\nv_add3_u32 v8, v118, v119, v71\nv_add3_u32 v6, v6, v7, v8\ns_nop 1\n" \
  "v_add_u32_dpp v6, v6, v6 quad_perm:[1,0,3,2] row_mask:0xf bank_mask:0xf\ns_nop 1\nv_add_u32_dpp v6, v6, v6 quad_perm:[2,3,0,1] row_mask:0xf bank_mask:0xf\n" \
  "v_cmp_ne_u32 vcc, s36, v6\ns_cbranch_vccz .Lfz_ma1\ns_sub_u32 s37, s37, 1\ns_cbranch_scc0 .Lfz_la1\n.Lfz_ma1:\n" \
  "v_mfma_f32_32x32x16_bf16 v[160:175], v[100:103], v[208:211], 0\ns_nop 6\ns_branch .Lfz_ka1\n.Lfz_xa1:\ns_nop 0\n" \
  "v_mfma_f32_32x32x16_bf16 v[144:159], v[64:67], v[204:207], v[144:159]\ns_branch .Lfz_xw0\n.Lfz_wda2:\ns_add_u32 s40, s31, 7\ns_and_b32 s40, s40, 7\n" \
  "s_lshl_b32 s40, s40, 16\ns_add_u32 m0, s3, 57344\ns_add_u32 s40, s12, s40\ns_addc_u32 s41, s13, 0\nglobal_load_lds_dwordx4 v10, s[40:41]\ns_add_u32 s40, s31, 6\n" \
  "s_and_b32 s40, s40, 7\ns_lshl_b32 s40, s40, 16\ns_add_u32 m0, s3, 49152\ns_add_u32 s40, s12, s40\ns_addc_u32 s41, s13, 0\nglobal_load_lds_dwordx4 v10, s[40:41]\n" \
  "s_add_u32 s40, s31, 5\ns_and_b32 s40, s40, 7\ns_lshl_b32 s40, s40, 16\ns_add_u32 m0, s3, 40960\ns_add_u32 s40, s12, s40\ns_addc_u32 s41, s13, 0\n" \
  "global_load_lds_dwordx4 v10, s[40:41]\ns_branch .Lfz_wdra2\n.Lfz_ra3:\ns_mov_b32 s37, 262144\n.Lfz_la3:\nglobal_load_dwordx4 v[96:99], v2, s[56:57] sc1\n" \
  "global_load_dwordx4 v[112:115], v2, s[56:57] offset:1024 sc1\ns_mov_b64 exec, s[50:51]\nglobal_load_dword v70, v3, s[56:57] sc1\ns_mov_b64 exec, -1\ns_waitcnt vmcnt(0)\n" \
  "v_add3_u32 v6, v96, v97, v98\nv_add3_u32 v7, v99, v112, v113\nv_add3_u32 v8, v114, v115, v70\nv_add3_u32 v6, v6, v7, v8\ns_nop 1\n" \
  "v_add_u32_dpp v6, v6, v6 quad_perm:[1,0,3,2] row_mask:0xf bank_mask:0xf\ns_nop 1\nv_add_u32_dpp v6, v6, v6 quad_perm:[2,3,0,1] row_mask:0xf bank_mask:0xf\n" \
  "v_cmp_ne_u32 vcc, s36, v6\ns_cbranch_vccz .Lfz_ma3\ns_sub_u32 s37, s37, 1\ns_cbranch_scc0 .Lfz_la3\n.Lfz_ma3:\n" \
  "v_mfma_f32_32x32x16_bf16 v[160:175], v[96:99], v[208:211], 0\ns_nop 6\ns_branch .Lfz_ka3\n.Lfz_xa3:\ns_nop 0\n" \
  "v_mfma_f32_32x32x16_bf16 v[144:159], v[60:63], v[204:207], v[144:159]\ns_branch .Lfz_xw0\n.Lfz_wda4:\ns_add_u32 s40, s31, 4\ns_and_b32 s40, s40, 7\n" \
  "s_lshl_b32 s40, s40, 16\ns_add_u32 m0, s3, 32768\ns_add_u32 s40, s12, s40\ns_addc_u32 s41, s13, 0\nglobal_load_lds_dwordx4 v10, s[40:41]\ns_add_u32 s40, s31, 3\n" \
  "s_and_b32 s40, s40, 7\ns_lshl_b32 s40, s40, 16\ns_add_u32 m0, s3, 24576\ns_add_u32 s40, s12, s40\ns_addc_u32 s41, s13, 0\nglobal_load_lds_dwordx4 v10, s[40:41]\n" \
  "s_add_u32 s40, s31, 2\ns_and_b32 s40, s40, 7\ns_lshl_b32 s40, s40, 16\ns_add_u32 m0, s3, 16384\ns_add_u32 s40, s12, s40\ns_addc_u32 s41, s13, 0\n" \
  "global_load_lds_dwordx4 v10, s[40:41]\ns_branch .Lfz_wdra4\n.Lfz_ra5:\ns_mov_b32 s37, 262144\n.Lfz_la5:\nglobal_load_dwordx4 v[92:95], v2, s[54:55] sc1\n" \
  "global_load_dwordx4 v[108:111], v2, s[54:55] offset:1024 sc1\ns_mov_b64 exec, s[50:51]\nglobal_load_dword v69, v3, s[54:55] sc1\ns_mov_b64 exec, -1\ns_waitcnt vmcnt(0)\n" \
  "v_add3_u32 v6, v92, v93, v94\nv_add3_u32 v7, v95, v108, v109\nv_add3_u32 v8, v110, v111, v69\nv_add3_u32 v6, v6, v7, v8\ns_nop 1\n" \
  "v_add_u32_dpp v6, v6, v6 quad_perm:[1,0,3,2] row_mask:0xf bank_mask:0xf\ns_nop 1\nv_add_u32_dpp v6, v6, v6 quad_perm:[2,3,0,1] row_mask:0xf bank_mask:0xf\n" \
  "v_cmp_ne_u32 vcc, s36, v6\ns_cbranch_vccz .Lfz_ma5\ns_sub_u32 s37, s37, 1\ns_cbranch_scc0 .Lfz_la5\n.Lfz_ma5:\n" \
  "v_mfma_f32_32x32x16_bf16 v[160:175], v[92:95], v[208:211], 0\ns_nop 6\ns_branch .Lfz_ka5\n.Lfz_xa5:\ns_nop 0\n" \
  "v_mfma_f32_32x32x16_bf16 v[144:159], v[56:59], v[204:207], v[144:159]\ns_branch .Lfz_xw0\n.Lfz_ra7:\ns_mov_b32 s37, 262144\n.Lfz_la7:\n" \
  "global_load_dwordx4 v[88:91], v2, s[52:53] sc1\nglobal_load_dwordx4 v[104:107], v2, s[52:53] offset:1024 sc1\ns_mov_b64 exec, s[50:51]\n" \
  "global_load_dword v68, v3, s[52:53] sc1\ns_mov_b64 exec, -1\ns_waitcnt vmcnt(0)\nv_add3_u32 v6, v88, v89, v90\nv_add3_u32 v7, v91, v104, v105\n" \
  "v_add3_u32 v8, v106, v107, v68\nv_add3_u32 v6, v6, v7, v8\ns_nop 1\nv_add_u32_dpp v6, v6, v6 quad_perm:[1,0,3,2] row_mask:0xf bank_mask:0xf\ns_nop 1\n" \
  "v_add_u32_dpp v6, v6, v6 quad_perm:[2,3,0,1] row_mask:0xf bank_mask:0xf\nv_cmp_ne_u32 vcc, s36, v6\ns_cbranch_vccz .Lfz_ma7\ns_sub_u32 s37, s37, 1\n" \
  "s_cbranch_scc0 .Lfz_la7\n.Lfz_ma7:\nv_mfma_f32_32x32x16_bf16 v[160:175], v[88:91], v[208:211], v[16:31]\ns_nop 6\ns_branch .Lfz_ka7\n.Lfz_xa7:\ns_nop 0\n" \
  "v_mfma_f32_32x32x16_bf16 v[144:159], v[52:55], v[204:207], v[144:159]\ns_branch .Lfz_xw0\n.Lfz_lt:\ns_lshl_b32 s3, s27, 10\ns_cmp_eq_u32 s28, 3\ns_cbranch_scc1 .Lfz_lz\n" \
  "s_cmp_eq_u32 s28, 2\ns_cbranch_scc1 .Lfz_lt1\ns_cmp_eq_u32 s28, 1\ns_cbranch_scc1 .Lfz_lt0\ns_add_u32 s40, s31, 7\ns_and_b32 s40, s40, 7\ns_lshl_b32 s40, s40, 16\n" \
  "s_add_u32 m0, s3, 57344\ns_add_u32 s40, s12, s40\ns_addc_u32 s41, s13, 0\nglobal_load_lds_dwordx4 v10, s[40:41]\ns_add_u32 s40, s31, 6\ns_and_b32 s40, s40, 7\n" \
  "s_lshl_b32 s40, s40, 16\ns_add_u32 m0, s3, 49152\ns_add_u32 s40, s12, s40\ns_addc_u32 s41, s13, 0\nglobal_load_lds_dwordx4 v10, s[40:41]\ns_add_u32 s40, s31, 5\n" \
  "s_and_b32 s40, s40, 7\ns_lshl_b32 s40, s40, 16\ns_add_u32 m0, s3, 40960\ns_add_u32 s40, s12, s40\ns_addc_u32 s41, s13, 0\nglobal_load_lds_dwordx4 v10, s[40:41]\n" \
  ".Lfz_lt0:\ns_add_u32 s40, s31, 4\ns_and_b32 s40, s40, 7\ns_lshl_b32 s40, s40, 16\ns_add_u32 m0, s3, 32768\ns_add_u32 s40, s12, s40\ns_addc_u32 s41, s13, 0\n" \
  "global_load_lds_dwordx4 v10, s[40:41]\ns_add_u32 s40, s31, 3\ns_and_b32 s40, s40, 7\ns_lshl_b32 s40, s40, 16\ns_add_u32 m0, s3, 24576\ns_add_u32 s40, s12, s40\n" \
  "s_addc_u32 s41, s13, 0\nglobal_load_lds_dwordx4 v10, s[40:41]\ns_add_u32 s40, s31, 2\ns_and_b32 s40, s40, 7\ns_lshl_b32 s40, s40, 16\ns_add_u32 m0, s3, 16384\n" \
  "s_add_u32 s40, s12, s40\ns_addc_u32 s41, s13, 0\nglobal_load_lds_dwordx4 v10, s[40:41]\n.Lfz_lt1:\ns_add_u32 s40, s31, 1\ns_and_b32 s40, s40, 7\ns_lshl_b32 s40, s40, 16\n" \
  "s_add_u32 m0, s3, 8192\ns_add_u32 s40, s12, s40\ns_addc_u32 s41, s13, 0\nglobal_load_lds_dwordx4 v10, s[40:41]\ns_add_u32 s40, s31, 0\ns_and_b32 s40, s40, 7\n" \
  "s_lshl_b32 s40, s40, 16\ns_add_u32 m0, s3, 0\ns_add_u32 s40, s12, s40\ns_addc_u32 s41, s13, 0\nglobal_load_lds_dwordx4 v10, s[40:41]\ns_branch .Lfz_b1\n.Lfz_lz:\n" \
  "s_add_u32 s40, s31, 7\ns_and_b32 s40, s40, 7\ns_lshl_b32 s40, s40, 16\ns_add_u32 m0, s3, 57344\ns_add_u32 s40, s12, s40\ns_addc_u32 s41, s13, 0\n" \
  "global_load_lds_dwordx4 v10, s[40:41]\ns_add_u32 s40, s31, 6\ns_and_b32 s40, s40, 7\ns_lshl_b32 s40, s40, 16\ns_add_u32 m0, s3, 49152\ns_add_u32 s40, s12, s40\n" \
  "s_addc_u32 s41, s13, 0\nglobal_load_lds_dwordx4 v10, s[40:41]\ns_waitcnt vmcnt(0) lgkmcnt(0)\ns_barrier\n.Lfz_b1r:\nds_read_b64_tr_b16 v[160:161], v76 offset:8192\n" \
  "ds_read_b64_tr_b16 v[162:163], v76 offset:9216\nds_read_b64_tr_b16 v[164:165], v77 offset:8192\nds_read_b64_tr_b16 v[166:167], v77 offset:9216\n" \
  "ds_read_b64_tr_b16 v[168:169], v78 offset:8192\nds_read_b64_tr_b16 v[170:171], v78 offset:9216\nds_read_b64_tr_b16 v[172:173], v79 offset:8192\n" \
  "ds_read_b64_tr_b16 v[174:175], v79 offset:9216\nds_read_b64_tr_b16 v[176:177], v76 offset:12288\nds_read_b64_tr_b16 v[178:179], v76 offset:13312\n" \
  "ds_read_b64_tr_b16 v[180:181], v77 offset:12288\nds_read_b64_tr_b16 v[182:183], v77 offset:13312\nds_read_b64_tr_b16 v[184:185], v78 offset:12288\n" \
  "ds_read_b64_tr_b16 v[186:187], v78 offset:13312\ns_branch .Lfz_br2\n.Lfz_slow:\ns_waitcnt vmcnt(0)\ns_lshl_b32 s45, s28, 1\ns_add_u32 s45, s45, s34\n" \
  "s_sub_u32 s45, 8, s45\ns_max_i32 s45, s45, 0\ns_mov_b32 s44, s45\nv_mov_b32 v244, 0\nv_mov_b32 v246, 0xff800000\n.Lfz_p1:\ns_add_u32 s40, s44, s34\n" \
  "s_lshr_b32 s41, s40, 1\ns_and_b32 s40, s40, 1\ns_sub_u32 s41, 4, s41\ns_mul_i32 s41, s41, 217088\ns_sub_u32 s42, s38, s41\ns_subb_u32 s43, s39, 0\n" \
  "s_lshl_b32 s41, s40, 2\ns_add_u32 s41, s41, s33\ns_lshl_b32 s41, s41, 11\ns_add_u32 s42, s42, s41\ns_addc_u32 s43, s43, 0\ns_sub_u32 s40, s34, s40\n" \
  "s_mul_i32 s40, s40, 7680\nv_add_u32 v245, s40, v3\ns_mov_b32 s37, 262144\n.Lfz_sl1:\nglobal_load_dwordx4 v[112:115], v2, s[42:43] sc1\n" \
  "global_load_dwordx4 v[116:119], v2, s[42:43] offset:1024 sc1\ns_mov_b64 exec, s[50:51]\nglobal_load_dword v244, v245, s[42:43] sc1\ns_mov_b64 exec, -1\n" \
  "s_waitcnt vmcnt(0)\nv_add3_u32 v6, v112, v113, v114\nv_add3_u32 v7, v115, v116, v117\nv_add3_u32 v8, v118, v119, v244\nv_add3_u32 v6, v6, v7, v8\ns_nop 1\n" \
  "v_add_u32_dpp v6, v6, v6 quad_perm:[1,0,3,2] row_mask:0xf bank_mask:0xf\ns_nop 1\nv_add_u32_dpp v6, v6, v6 quad_perm:[2,3,0,1] row_mask:0xf bank_mask:0xf\n" \
  "v_cmp_ne_u32 vcc, s36, v6\ns_cbranch_vccz .Lfz_sk1\ns_sub_u32 s37, s37, 1\ns_cbranch_scc0 .Lfz_sl1\n.Lfz_sk1:\n" \
  "v_mfma_f32_32x32x16_bf16 v[176:191], v[112:115], v[208:211], 0\ns_nop 11\ns_and_b32 s40, s44, 7\ns_cbranch_scc1 .Lfz_sn1\ns_cmp_eq_u32 s44, 8\n" \
  "s_cselect_b32 s40, 0xff800000, 0\nv_xor_b32 v6, s40, v16\nv_add_f32 v176, v176, v6\nv_xor_b32 v6, s40, v17\nv_add_f32 v177, v177, v6\nv_xor_b32 v6, s40, v18\n" \
  "v_add_f32 v178, v178, v6\nv_xor_b32 v6, s40, v19\nv_add_f32 v179, v179, v6\nv_xor_b32 v6, s40, v20\nv_add_f32 v180, v180, v6\nv_xor_b32 v6, s40, v21\n" \
  "v_add_f32 v181, v181, v6\nv_xor_b32 v6, s40, v22\nv_add_f32 v182, v182, v6\nv_xor_b32 v6, s40, v23\nv_add_f32 v183, v183, v6\nv_xor_b32 v6, s40, v24\n" \
  "v_add_f32 v184, v184, v6\nv_xor_b32 v6, s40, v25\nv_add_f32 v185, v185, v6\nv_xor_b32 v6, s40, v26\nv_add_f32 v186, v186, v6\nv_xor_b32 v6, s40, v27\n" \
  "v_add_f32 v187, v187, v6\nv_xor_b32 v6, s40, v28\nv_add_f32 v188, v188, v6\nv_xor_b32 v6, s40, v29\nv_add_f32 v189, v189, v6\nv_xor_b32 v6, s40, v30\n" \
  "v_add_f32 v190, v190, v6\nv_xor_b32 v6, s40, v31\nv_add_f32 v191, v191, v6\n.Lfz_sn1:\nv_max3_f32 v246, v246, v176, v177\nv_max3_f32 v246, v246, v178, v179\n" \
  "v_max3_f32 v246, v246, v180, v181\nv_max3_f32 v246, v246, v182, v183\nv_max3_f32 v246, v246, v184, v185\nv_max3_f32 v246, v246, v186, v187\n" \
  "v_max3_f32 v246, v246, v188, v189\nv_max3_f32 v246, v246, v190, v191\ns_add_u32 s44, s44, 1\ns_cmp_le_u32 s44, 8\ns_cbranch_scc1 .Lfz_p1\nv_mov_b32 v6, v246\n" \
  "s_mov_b32 s44, s45\nv_mov_b64 v[128:129], 0\nv_mov_b64 v[130:131], 0\nv_mov_b64 v[132:133], 0\nv_mov_b64 v[134:135], 0\nv_mov_b64 v[136:137], 0\nv_mov_b64 v[138:139], 0\n" \
  "v_mov_b64 v[140:141], 0\nv_mov_b64 v[142:143], 0\nv_permlane32_swap_b32 v6, v246\ns_nop 0\nv_max_f32 v246, v246, v6\n.Lfz_p2:\ns_add_u32 s40, s44, s34\n" \
  "s_lshr_b32 s41, s40, 1\ns_and_b32 s40, s40, 1\ns_sub_u32 s41, 4, s41\ns_mul_i32 s41, s41, 217088\ns_sub_u32 s42, s38, s41\ns_subb_u32 s43, s39, 0\n" \
  "s_lshl_b32 s41, s40, 2\ns_add_u32 s41, s41, s33\ns_lshl_b32 s41, s41, 11\ns_add_u32 s42, s42, s41\ns_addc_u32 s43, s43, 0\ns_sub_u32 s40, s34, s40\n" \
  "s_mul_i32 s40, s40, 7680\nv_add_u32 v245, s40, v3\ns_mov_b32 s37, 262144\n.Lfz_sl2:\nglobal_load_dwordx4 v[112:115], v2, s[42:43] sc1\n" \
  "global_load_dwordx4 v[116:119], v2, s[42:43] offset:1024 sc1\ns_mov_b64 exec, s[50:51]\nglobal_load_dword v244, v245, s[42:43] sc1\ns_mov_b64 exec, -1\n" \
  "s_waitcnt vmcnt(0)\nv_add3_u32 v6, v112, v113, v114\nv_add3_u32 v7, v115, v116, v117\nv_add3_u32 v8, v118, v119, v244\nv_add3_u32 v6, v6, v7, v8\ns_nop 1\n" \
  "v_add_u32_dpp v6, v6, v6 quad_perm:[1,0,3,2] row_mask:0xf bank_mask:0xf\ns_nop 1\nv_add_u32_dpp v6, v6, v6 quad_perm:[2,3,0,1] row_mask:0xf bank_mask:0xf\n" \
  "v_cmp_ne_u32 vcc, s36, v6\ns_cbranch_vccz .Lfz_sk2\ns_sub_u32 s37, s37, 1\ns_cbranch_scc0 .Lfz_sl2\n.Lfz_sk2:\n" \
  "v_mfma_f32_32x32x16_bf16 v[176:191], v[112:115], v[208:211], 0\ns_nop 11\ns_and_b32 s40, s44, 7\ns_cbranch_scc1 .Lfz_sn2\ns_cmp_eq_u32 s44, 8\n" \
  "s_cselect_b32 s40, 0xff800000, 0\nv_xor_b32 v6, s40, v16\nv_add_f32 v176, v176, v6\nv_xor_b32 v6, s40, v17\nv_add_f32 v177, v177, v6\nv_xor_b32 v6, s40, v18\n" \
  "v_add_f32 v178, v178, v6\nv_xor_b32 v6, s40, v19\nv_add_f32 v179, v179, v6\nv_xor_b32 v6, s40, v20\nv_add_f32 v180, v180, v6\nv_xor_b32 v6, s40, v21\n" \
  "v_add_f32 v181, v181, v6\nv_xor_b32 v6, s40, v22\nv_add_f32 v182, v182, v6\nv_xor_b32 v6, s40, v23\nv_add_f32 v183, v183, v6\nv_xor_b32 v6, s40, v24\n" \
  "v_add_f32 v184, v184, v6\nv_xor_b32 v6, s40, v25\nv_add_f32 v185, v185, v6\nv_xor_b32 v6, s40, v26\nv_add_f32 v186, v186, v6\nv_xor_b32 v6, s40, v27\n" \
  "v_add_f32 v187, v187, v6\nv_xor_b32 v6, s40, v28\nv_add_f32 v188, v188, v6\nv_xor_b32 v6, s40, v29\nv_add_f32 v189, v189, v6\nv_xor_b32 v6, s40, v30\n" \
  "v_add_f32 v190, v190, v6\nv_xor_b32 v6, s40, v31\nv_add_f32 v191, v191, v6\n.Lfz_sn2:\nv_sub_f32 v176, v176, v246\nv_sub_f32 v177, v177, v246\n" \
  "v_sub_f32 v178, v178, v246\nv_sub_f32 v179, v179, v246\nv_sub_f32 v180, v180, v246\nv_sub_f32 v181, v181, v246\nv_sub_f32 v182, v182, v246\nv_sub_f32 v183, v183, v246\n" \
  "v_sub_f32 v184, v184, v246\nv_sub_f32 v185, v185, v246\nv_sub_f32 v186, v186, v246\nv_sub_f32 v187, v187, v246\nv_sub_f32 v188, v188, v246\nv_sub_f32 v189, v189, v246\n" \
  "v_sub_f32 v190, v190, v246\nv_sub_f32 v191, v191, v246\nv_exp_f32 v176, v176\nv_exp_f32 v177, v177\nv_exp_f32 v178, v178\nv_exp_f32 v179, v179\nv_exp_f32 v180, v180\n" \
  "v_exp_f32 v181, v181\nv_exp_f32 v182, v182\nv_exp_f32 v183, v183\nv_exp_f32 v184, v184\nv_exp_f32 v185, v185\nv_exp_f32 v186, v186\nv_exp_f32 v187, v187\n" \
  "v_exp_f32 v188, v188\nv_exp_f32 v189, v189\nv_exp_f32 v190, v190\nv_exp_f32 v191, v191\nv_mov_b32 v240, 0x3f803f80\nv_mov_b32 v241, 0x3f803f80\n" \
  "v_mov_b32 v242, 0x3f803f80\nv_mov_b32 v243, 0x3f803f80\nv_cvt_pk_bf16_f32 v192, v176, v177\nv_cvt_pk_bf16_f32 v193, v178, v179\nv_cvt_pk_bf16_f32 v194, v180, v181\n" \
  "v_cvt_pk_bf16_f32 v195, v182, v183\nv_cvt_pk_bf16_f32 v196, v184, v185\nv_cvt_pk_bf16_f32 v197, v186, v187\nv_cvt_pk_bf16_f32 v198, v188, v189\n" \
  "v_cvt_pk_bf16_f32 v199, v190, v191\nv_permlane16_swap_b32 v116, v240\nv_permlane16_swap_b32 v117, v241\nv_permlane16_swap_b32 v118, v242\n" \
  "v_permlane16_swap_b32 v119, v243\ns_nop 1\nv_mfma_f32_32x32x16_bf16 v[128:143], v[116:119], v[192:195], v[128:143]\n" \
  "v_mfma_f32_32x32x16_bf16 v[128:143], v[240:243], v[196:199], v[128:143]\ns_add_u32 s44, s44, 1\ns_cmp_le_u32 s44, 8\ns_cbranch_scc1 .Lfz_p2\ns_nop 11\n" \
  "v_rcp_f32 v6, v136\ns_nop 0\nv_pk_mul_f32 v[128:129], v[128:129], v[6:7] op_sel_hi:[1,0]\nv_pk_mul_f32 v[130:131], v[130:131], v[6:7] op_sel_hi:[1,0]\n" \
  "v_pk_mul_f32 v[132:133], v[132:133], v[6:7] op_sel_hi:[1,0]\nv_pk_mul_f32 v[134:135], v[134:135], v[6:7] op_sel_hi:[1,0]\nv_cvt_pk_bf16_f32 v120, v128, v129\n" \
  "v_cvt_pk_bf16_f32 v121, v130, v131\nv_cvt_pk_bf16_f32 v122, v132, v133\nv_cvt_pk_bf16_f32 v123, v134, v135\nglobal_store_dwordx4 v11, v[120:123], s[38:39] sc1\n" \
  "s_branch .Lfz_slowr\n.Lfz_st:\ns_mov_b32 s37, 262144\n.Lfz_stl:\nglobal_load_dwordx4 v[220:223], v15, s[60:61] sc1\ns_waitcnt vmcnt(0)\n" \
  "v_bitop3_b32 v6, v221, v223, s48 bitop3:0x7e\nv_cmp_ne_u32 vcc, 0, v6\ns_cbranch_vccz .Lfz_str\ns_sub_u32 s37, s37, 1\ns_cbranch_scc0 .Lfz_stl\ns_branch .Lfz_str\n" \
  ".Lfz_ort0:\nglobal_load_dwordx4 v[88:91], v2, s[60:61] sc1\nglobal_load_dwordx4 v[92:95], v2, s[60:61] offset:1024 sc1\ns_mov_b64 exec, s[50:51]\n" \
  "global_load_dword v72, v85, s[60:61] sc1\ns_mov_b64 exec, -1\ns_bitset1_b32 s5, 0\ns_branch .Lfz_ook0\n.Lfz_ort1:\nglobal_load_dwordx4 v[96:99], v2, s[62:63] sc1\n" \
  "global_load_dwordx4 v[100:103], v2, s[62:63] offset:1024 sc1\ns_mov_b64 exec, s[50:51]\nglobal_load_dword v73, v85, s[62:63] sc1\ns_mov_b64 exec, -1\n" \
  "s_bitset1_b32 s5, 1\ns_branch .Lfz_ook1\n.Lfz_ort2:\nglobal_load_dwordx4 v[104:107], v2, s[64:65] sc1\nglobal_load_dwordx4 v[108:111], v2, s[64:65] offset:1024 sc1\n" \
  "s_mov_b64 exec, s[50:51]\nglobal_load_dword v74, v85, s[64:65] sc1\ns_mov_b64 exec, -1\ns_bitset1_b32 s5, 2\ns_branch .Lfz_ook2\n.Lfz_ort3:\n" \
  "global_load_dwordx4 v[112:115], v2, s[66:67] sc1\nglobal_load_dwordx4 v[116:119], v2, s[66:67] offset:1024 sc1\ns_mov_b64 exec, s[50:51]\n" \
  "global_load_dword v75, v85, s[66:67] sc1\ns_mov_b64 exec, -1\ns_bitset1_b32 s5, 3\ns_branch .Lfz_ook3\n.Lfz_op:\ns_mov_b32 s37, 262144\ns_mov_b32 s4, s5\n.Lfz_opl:\n" \
  "s_waitcnt vmcnt(0)\ns_bitcmp1_b32 s5, 0\ns_cbranch_scc0 .Lfz_on0\nv_add3_u32 v6, v88, v89, v90\nv_add3_u32 v7, v91, v92, v93\nv_add3_u32 v8, v94, v95, v72\n" \
  "v_add3_u32 v6, v6, v7, v8\ns_nop 1\nv_add_u32_dpp v6, v6, v6 quad_perm:[1,0,3,2] row_mask:0xf bank_mask:0xf\ns_nop 1\n" \
  "v_add_u32_dpp v6, v6, v6 quad_perm:[2,3,0,1] row_mask:0xf bank_mask:0xf\nv_cmp_ne_u32 vcc, s36, v6\ns_cbranch_vccz .Lfz_og0\ns_cmp_eq_u32 s37, 0\n" \
  "s_cbranch_scc1 .Lfz_og0\nglobal_load_dwordx4 v[88:91], v2, s[60:61] sc1\nglobal_load_dwordx4 v[92:95], v2, s[60:61] offset:1024 sc1\ns_mov_b64 exec, s[50:51]\n" \
  "global_load_dword v72, v85, s[60:61] sc1\ns_mov_b64 exec, -1\ns_branch .Lfz_on0\n.Lfz_og0:\nv_mfma_f32_16x16x32_bf16 v[136:139], v[136:139], v[88:91], 0\n" \
  "v_mfma_f32_16x16x32_bf16 v[140:143], v[140:143], v[88:91], 0\nv_mfma_f32_16x16x32_bf16 v[136:139], v[152:155], v[92:95], v[136:139]\n" \
  "v_mfma_f32_16x16x32_bf16 v[140:143], v[156:159], v[92:95], v[140:143]\nv_mfma_f32_16x16x32_bf16 v[128:131], v[128:131], v[88:91], 0\n" \
  "v_mfma_f32_16x16x32_bf16 v[132:135], v[132:135], v[88:91], 0\nv_mfma_f32_16x16x32_bf16 v[128:131], v[144:147], v[92:95], v[128:131]\n" \
  "v_mfma_f32_16x16x32_bf16 v[132:135], v[148:151], v[92:95], v[132:135]\ns_bitset0_b32 s5, 0\n.Lfz_on0:\ns_bitcmp1_b32 s5, 1\ns_cbranch_scc0 .Lfz_on1\n" \
  "v_add3_u32 v6, v96, v97, v98\nv_add3_u32 v7, v99, v100, v101\nv_add3_u32 v8, v102, v103, v73\nv_add3_u32 v6, v6, v7, v8\ns_nop 1\n" \
  "v_add_u32_dpp v6, v6, v6 quad_perm:[1,0,3,2] row_mask:0xf bank_mask:0xf\ns_nop 1\nv_add_u32_dpp v6, v6, v6 quad_perm:[2,3,0,1] row_mask:0xf bank_mask:0xf\n" \
  "v_cmp_ne_u32 vcc, s36, v6\ns_cbranch_vccz .Lfz_og1\ns_cmp_eq_u32 s37, 0\ns_cbranch_scc1 .Lfz_og1\nglobal_load_dwordx4 v[96:99], v2, s[62:63] sc1\n" \
  "global_load_dwordx4 v[100:103], v2, s[62:63] offset:1024 sc1\ns_mov_b64 exec, s[50:51]\nglobal_load_dword v73, v85, s[62:63] sc1\ns_mov_b64 exec, -1\ns_branch .Lfz_on1\n" \
  ".Lfz_og1:\nv_mfma_f32_16x16x32_bf16 v[168:171], v[168:171], v[96:99], 0\nv_mfma_f32_16x16x32_bf16 v[172:175], v[172:175], v[96:99], 0\n" \
  "v_mfma_f32_16x16x32_bf16 v[168:171], v[184:187], v[100:103], v[168:171]\nv_mfma_f32_16x16x32_bf16 v[172:175], v[188:191], v[100:103], v[172:175]\n" \
  "v_mfma_f32_16x16x32_bf16 v[160:163], v[160:163], v[96:99], 0\nv_mfma_f32_16x16x32_bf16 v[164:167], v[164:167], v[96:99], 0\n" \
  "v_mfma_f32_16x16x32_bf16 v[160:163], v[176:179], v[100:103], v[160:163]\nv_mfma_f32_16x16x32_bf16 v[164:167], v[180:183], v[100:103], v[164:167]\ns_bitset0_b32 s5, 1\n" \
  ".Lfz_on1:\ns_bitcmp1_b32 s5, 2\ns_cbranch_scc0 .Lfz_on2\nv_add3_u32 v6, v104, v105, v106\nv_add3_u32 v7, v107, v108, v109\nv_add3_u32 v8, v110, v111, v74\n" \
  "v_add3_u32 v6, v6, v7, v8\ns_nop 1\nv_add_u32_dpp v6, v6, v6 quad_perm:[1,0,3,2] row_mask:0xf bank_mask:0xf\ns_nop 1\n" \
  "v_add_u32_dpp v6, v6, v6 quad_perm:[2,3,0,1] row_mask:0xf bank_mask:0xf\nv_cmp_ne_u32 vcc, s36, v6\ns_cbranch_vccz .Lfz_og2\ns_cmp_eq_u32 s37, 0\n" \
  "s_cbranch_scc1 .Lfz_og2\nglobal_load_dwordx4 v[104:107], v2, s[64:65] sc1\nglobal_load_dwordx4 v[108:111], v2, s[64:65] offset:1024 sc1\ns_mov_b64 exec, s[50:51]\n" \
  "global_load_dword v74, v85, s[64:65] sc1\ns_mov_b64 exec, -1\ns_branch .Lfz_on2\n.Lfz_og2:\nv_mfma_f32_16x16x32_bf16 v[200:203], v[200:203], v[104:107], 0\n" \
  "v_mfma_f32_16x16x32_bf16 v[204:207], v[204:207], v[104:107], 0\nv_mfma_f32_16x16x32_bf16 v[200:203], v[216:219], v[108:111], v[200:203]\n" \
  "v_mfma_f32_16x16x32_bf16 v[204:207], v[220:223], v[108:111], v[204:207]\nv_mfma_f32_16x16x32_bf16 v[192:195], v[192:195], v[104:107], 0\n" \
  "v_mfma_f32_16x16x32_bf16 v[196:199], v[196:199], v[104:107], 0\nv_mfma_f32_16x16x32_bf16 v[192:195], v[208:211], v[108:111], v[192:195]\n" \
  "v_mfma_f32_16x16x32_bf16 v[196:199], v[212:215], v[108:111], v[196:199]\ns_bitset0_b32 s5, 2\n.Lfz_on2:\ns_bitcmp1_b32 s5, 3\ns_cbranch_scc0 .Lfz_on3\n" \
  "v_add3_u32 v6, v112, v113, v114\nv_add3_u32 v7, v115, v116, v117\nv_add3_u32 v8, v118, v119, v75\nv_add3_u32 v6, v6, v7, v8\ns_nop 1\n" \
  "v_add_u32_dpp v6, v6, v6 quad_perm:[1,0,3,2] row_mask:0xf bank_mask:0xf\ns_nop 1\nv_add_u32_dpp v6, v6, v6 quad_perm:[2,3,0,1] row_mask:0xf bank_mask:0xf\n" \
  "v_cmp_ne_u32 vcc, s36, v6\ns_cbranch_vccz .Lfz_og3\ns_cmp_eq_u32 s37, 0\ns_cbranch_scc1 .Lfz_og3\nglobal_load_dwordx4 v[112:115], v2, s[66:67] sc1\n" \
  "global_load_dwordx4 v[116:119], v2, s[66:67] offset:1024 sc1\ns_mov_b64 exec, s[50:51]\nglobal_load_dword v75, v85, s[66:67] sc1\ns_mov_b64 exec, -1\ns_branch .Lfz_on3\n" \
  ".Lfz_og3:\nv_mfma_f32_16x16x32_bf16 v[232:235], v[232:235], v[112:115], 0\nv_mfma_f32_16x16x32_bf16 v[236:239], v[236:239], v[112:115], 0\n" \
  "v_mfma_f32_16x16x32_bf16 v[232:235], v[248:251], v[116:119], v[232:235]\nv_mfma_f32_16x16x32_bf16 v[236:239], v[252:255], v[116:119], v[236:239]\n" \
  "v_mfma_f32_16x16x32_bf16 v[224:227], v[224:227], v[112:115], 0\nv_mfma_f32_16x16x32_bf16 v[228:231], v[228:231], v[112:115], 0\n" \
  "v_mfma_f32_16x16x32_bf16 v[224:227], v[240:243], v[116:119], v[224:227]\nv_mfma_f32_16x16x32_bf16 v[228:231], v[244:247], v[116:119], v[228:231]\ns_bitset0_b32 s5, 3\n" \
  ".Lfz_on3:\ns_sub_u32 s37, s37, 1\ns_cmp_lg_u32 s5, 0\ns_cbranch_scc1 .Lfz_opl\ns_nop 3\ns_and_b32 s40, s4, 1\ns_cbranch_scc0 .Lfz_oq1\n" \
  "v_pk_add_f32 v[40:41], v[40:41], v[136:137]\nv_pk_add_f32 v[42:43], v[42:43], v[138:139]\nv_pk_add_f32 v[44:45], v[44:45], v[140:141]\n" \
  "v_pk_add_f32 v[46:47], v[46:47], v[142:143]\nv_pk_add_f32 v[32:33], v[32:33], v[128:129]\nv_pk_add_f32 v[34:35], v[34:35], v[130:131]\n" \
  "v_pk_add_f32 v[36:37], v[36:37], v[132:133]\nv_pk_add_f32 v[38:39], v[38:39], v[134:135]\n.Lfz_oq1:\ns_and_b32 s40, s4, 3\ns_cbranch_scc0 .Lfz_oq2\n" \
  "v_pk_add_f32 v[40:41], v[40:41], v[168:169]\nv_pk_add_f32 v[42:43], v[42:43], v[170:171]\nv_pk_add_f32 v[44:45], v[44:45], v[172:173]\n" \
  "v_pk_add_f32 v[46:47], v[46:47], v[174:175]\nv_pk_add_f32 v[32:33], v[32:33], v[160:161]\nv_pk_add_f32 v[34:35], v[34:35], v[162:163]\n" \
  "v_pk_add_f32 v[36:37], v[36:37], v[164:165]\nv_pk_add_f32 v[38:39], v[38:39], v[166:167]\n.Lfz_oq2:\ns_and_b32 s40, s4, 7\ns_cbranch_scc0 .Lfz_opr\n" \
  "v_pk_add_f32 v[40:41], v[40:41], v[200:201]\nv_pk_add_f32 v[42:43], v[42:43], v[202:203]\nv_pk_add_f32 v[44:45], v[44:45], v[204:205]\n" \
  "v_pk_add_f32 v[46:47], v[46:47], v[206:207]\nv_pk_add_f32 v[32:33], v[32:33], v[192:193]\nv_pk_add_f32 v[34:35], v[34:35], v[194:195]\n" \
  "v_pk_add_f32 v[36:37], v[36:37], v[196:197]\nv_pk_add_f32 v[38:39], v[38:39], v[198:199]\ns_branch .Lfz_opr\n.Lfz_y0:\ns_lshl_b32 s40, s2, 8\ns_lshl_b32 s41, s34, 7\n" \
  "s_add_u32 s40, s40, s41\ns_add_u32 s40, s40, 524288\ns_add_u32 s44, s24, s40\ns_addc_u32 s45, s25, 0\nv_and_b32 v6, 15, v1\nv_and_b32 v7, 48, v1\n" \
  "v_lshl_or_b32 v64, v6, 11, v7\nv_add_u32 v65, 32768, v64\nv_mov_b32 v56, 0\nv_mov_b32 v57, s48\nv_mov_b32 v58, 0\nv_mov_b32 v59, s48\nv_mov_b32 v60, 0\n" \
  "v_mov_b32 v61, s48\nv_mov_b32 v62, 0\nv_mov_b32 v63, s48\nglobal_load_dwordx4 v[48:51], v64, s[44:45] sc1\nglobal_load_dwordx4 v[52:55], v64, s[44:45] offset:64 sc1\n" \
  "s_mov_b32 exec_lo, 0x00ff00ff\ns_mov_b32 exec_hi, 0x00ff00ff\nglobal_load_dwordx4 v[56:59], v65, s[44:45] sc1\nglobal_load_dwordx4 v[60:63], v65, s[44:45] offset:64 sc1\n" \
  "s_mov_b64 exec, -1\ns_branch .Lfz_y0r\n.Lfz_yl:\nglobal_load_dwordx4 v[48:51], v64, s[44:45] sc1\nglobal_load_dwordx4 v[52:55], v64, s[44:45] offset:64 sc1\n" \
  "s_mov_b32 exec_lo, 0x00ff00ff\ns_mov_b32 exec_hi, 0x00ff00ff\nglobal_load_dwordx4 v[56:59], v65, s[44:45] sc1\nglobal_load_dwordx4 v[60:63], v65, s[44:45] offset:64 sc1\n" \
  "s_mov_b64 exec, -1\ns_waitcnt vmcnt(0)\ns_branch .Lfz_yc\n.Lfz_y:\ns_mov_b32 s37, 262144\ns_waitcnt vmcnt(0)\n.Lfz_yc:\nv_bitop3_b32 v88, v49, v51, s48 bitop3:0x7e\n" \
  "v_bitop3_b32 v89, v53, v55, s48 bitop3:0x7e\nv_bitop3_b32 v90, v57, v59, s48 bitop3:0x7e\nv_bitop3_b32 v91, v61, v63, s48 bitop3:0x7e\nv_or3_b32 v6, v88, v89, v90\n" \
  "v_or_b32 v6, v6, v91\nv_cmp_ne_u32 vcc, 0, v6\ns_cbranch_vccz .Lfz_yk\ns_sub_u32 s37, s37, 1\ns_cbranch_scc0 .Lfz_yl\n.Lfz_yk:\nv_lshlrev_b32 v88, 16, v48\n" \
  "v_and_b32 v89, 0xffff0000, v48\nv_lshlrev_b32 v90, 16, v50\nv_and_b32 v91, 0xffff0000, v50\nv_lshlrev_b32 v92, 16, v52\nv_and_b32 v93, 0xffff0000, v52\n" \
  "v_lshlrev_b32 v94, 16, v54\nv_and_b32 v95, 0xffff0000, v54\nv_lshlrev_b32 v96, 16, v56\nv_and_b32 v97, 0xffff0000, v56\nv_lshlrev_b32 v98, 16, v58\n" \
  "v_and_b32 v99, 0xffff0000, v58\nv_lshlrev_b32 v100, 16, v60\nv_and_b32 v101, 0xffff0000, v60\nv_lshlrev_b32 v102, 16, v62\nv_and_b32 v103, 0xffff0000, v62\n" \
  "v_add_f32 v88, v88, v96\nv_add_f32 v89, v89, v97\nv_add_f32 v90, v90, v98\nv_add_f32 v91, v91, v99\nv_add_f32 v92, v92, v100\nv_add_f32 v93, v93, v101\n" \
  "v_add_f32 v94, v94, v102\nv_add_f32 v95, v95, v103\nv_add_f32_dpp v88, v88, v88 quad_perm:[1,0,3,2] row_mask:0xf bank_mask:0xf\n" \
  "v_add_f32_dpp v89, v89, v89 quad_perm:[1,0,3,2] row_mask:0xf bank_mask:0xf\nv_add_f32_dpp v90, v90, v90 quad_perm:[1,0,3,2] row_mask:0xf bank_mask:0xf\n" \
  "v_add_f32_dpp v91, v91, v91 quad_perm:[1,0,3,2] row_mask:0xf bank_mask:0xf\nv_add_f32_dpp v92, v92, v92 quad_perm:[1,0,3,2] row_mask:0xf bank_mask:0xf\n" \
  "v_add_f32_dpp v93, v93, v93 quad_perm:[1,0,3,2] row_mask:0xf bank_mask:0xf\nv_add_f32_dpp v94, v94, v94 quad_perm:[1,0,3,2] row_mask:0xf bank_mask:0xf\n" \
  "v_add_f32_dpp v95, v95, v95 quad_perm:[1,0,3,2] row_mask:0xf bank_mask:0xf\nv_add_f32_dpp v88, v88, v88 quad_perm:[2,3,0,1] row_mask:0xf bank_mask:0xf\n" \
  "v_add_f32_dpp v89, v89, v89 quad_perm:[2,3,0,1] row_mask:0xf bank_mask:0xf\nv_add_f32_dpp v90, v90, v90 quad_perm:[2,3,0,1] row_mask:0xf bank_mask:0xf\n" \
  "v_add_f32_dpp v91, v91, v91 quad_perm:[2,3,0,1] row_mask:0xf bank_mask:0xf\nv_add_f32_dpp v92, v92, v92 quad_perm:[2,3,0,1] row_mask:0xf bank_mask:0xf\n" \
  "v_add_f32_dpp v93, v93, v93 quad_perm:[2,3,0,1] row_mask:0xf bank_mask:0xf\nv_add_f32_dpp v94, v94, v94 quad_perm:[2,3,0,1] row_mask:0xf bank_mask:0xf\n" \
  "v_add_f32_dpp v95, v95, v95 quad_perm:[2,3,0,1] row_mask:0xf bank_mask:0xf\nv_add_f32_dpp v88, v88, v88 row_half_mirror row_mask:0xf bank_mask:0xf\n" \
  "v_add_f32_dpp v89, v89, v89 row_half_mirror row_mask:0xf bank_mask:0xf\nv_add_f32_dpp v90, v90, v90 row_half_mirror row_mask:0xf bank_mask:0xf\n" \
  "v_add_f32_dpp v91, v91, v91 row_half_mirror row_mask:0xf bank_mask:0xf\nv_add_f32_dpp v92, v92, v92 row_half_mirror row_mask:0xf bank_mask:0xf\n" \
  "v_add_f32_dpp v93, v93, v93 row_half_mirror row_mask:0xf bank_mask:0xf\nv_add_f32_dpp v94, v94, v94 row_half_mirror row_mask:0xf bank_mask:0xf\n" \
  "v_add_f32_dpp v95, v95, v95 row_half_mirror row_mask:0xf bank_mask:0xf\nv_add_f32_dpp v88, v88, v88 row_mirror row_mask:0xf bank_mask:0xf\n" \
  "v_add_f32_dpp v89, v89, v89 row_mirror row_mask:0xf bank_mask:0xf\nv_add_f32_dpp v90, v90, v90 row_mirror row_mask:0xf bank_mask:0xf\n" \
  "v_add_f32_dpp v91, v91, v91 row_mirror row_mask:0xf bank_mask:0xf\nv_add_f32_dpp v92, v92, v92 row_mirror row_mask:0xf bank_mask:0xf\n" \
  "v_add_f32_dpp v93, v93, v93 row_mirror row_mask:0xf bank_mask:0xf\nv_add_f32_dpp v94, v94, v94 row_mirror row_mask:0xf bank_mask:0xf\n" \
  "v_add_f32_dpp v95, v95, v95 row_mirror row_mask:0xf bank_mask:0xf\ns_andn2_b32 s40, 15, s10\ns_lshl_b32 s40, 0x10001, s40\ns_mov_b32 exec_lo, s40\n" \
  "s_mov_b32 exec_hi, s40\nv_add_f32 v32, v32, v88\nv_add_f32 v33, v33, v89\nv_add_f32 v34, v34, v90\nv_add_f32 v35, v35, v91\nv_add_f32 v36, v36, v92\n" \
  "v_add_f32 v37, v37, v93\nv_add_f32 v38, v38, v94\nv_add_f32 v39, v39, v95\ns_mov_b64 exec, -1\ns_branch .Lfz_yr\n.Lfz_b1:\ns_cmp_gt_u32 s11, s27\n" \
  "s_cbranch_scc0 .Lfz_b11\ns_andn2_b32 s40, 2040, s10\ns_add_u32 s40, s40, 6144\nv_mov_b32 v66, s40\ns_add_u32 s40, s14, s27\ns_lshl_b32 s40, s40, 13\n" \
  "s_add_u32 s40, s40, 69632\ns_add_u32 s46, s24, s40\ns_addc_u32 s47, s25, 0\nglobal_load_dwordx2 v[64:65], v66, s[24:25] sc1\n" \
  "global_load_dwordx4 v[16:19], v2, s[46:47] offset:-4096 sc1\nglobal_load_dwordx4 v[20:23], v2, s[46:47] offset:-3072 sc1\n" \
  "global_load_dwordx4 v[24:27], v2, s[46:47] offset:-2048 sc1\nglobal_load_dwordx4 v[28:31], v2, s[46:47] offset:-1024 sc1\n" \
  "global_load_dwordx4 v[48:51], v2, s[46:47] offset:0 sc1\nglobal_load_dwordx4 v[52:55], v2, s[46:47] offset:1024 sc1\n" \
  "global_load_dwordx4 v[56:59], v2, s[46:47] offset:2048 sc1\nglobal_load_dwordx4 v[60:63], v2, s[46:47] offset:3072 sc1\n.Lfz_b11:\ns_and_b32 s40, s10, 0xffff0000\n" \
  "s_xor_b32 s41, s40, 0x80000000\ns_xor_b32 s42, s40, 0xffff0000\ns_cmp_lt_i32 s40, 0\ns_cselect_b32 s40, s41, s42\nv_mov_b32 v84, 0xbfb8aa3b\nv_mul_f32 v84, s40, v84\n" \
  "v_exp_f32 v84, v84\nv_bfe_u32 v6, v1, 2, 2\nv_add_f32 v84, 1.0, v84\nv_rcp_f32 v84, v84\ns_lshl_b32 s40, s27, 1\nv_xor_b32 v7, s40, v6\nv_cvt_pk_bf16_f32 v84, v84, v84\n" \
  "v_and_b32 v84, 0xffff0000, v84\nv_lshlrev_b32 v6, 10, v6\nv_lshl_or_b32 v6, v7, 6, v6\nv_bfe_u32 v7, v1, 4, 1\nv_lshl_or_b32 v6, v7, 5, v6\nv_and_b32 v7, 3, v1\n" \
  "v_lshl_or_b32 v6, v7, 3, v6\nv_lshrrev_b32 v7, 5, v1\nv_lshl_or_b32 v6, v7, 13, v6\nv_add_u32 v80, 73728, v6\nv_xor_b32 v81, 64, v80\nv_lshlrev_b32 v7, 4, v7\n" \
  "v_add_u32 v87, 155776, v7\ns_lshl_b32 s40, s28, 3\ns_add_u32 s40, s40, s29\ns_lshl_b32 s40, s40, 11\ns_lshl_b32 s41, s27, 8\ns_add_u32 s40, s40, s41\n" \
  "s_add_u32 s40, s40, 524288\nv_lshl_add_u32 v86, v1, 2, s40\nv_mov_b32 v121, s48\nv_mov_b32 v123, s48\ns_waitcnt vmcnt(0)\ns_cmp_gt_u32 s11, s27\ns_cbranch_scc0 .Lfz_b14\n" \
  "s_mov_b32 s37, 262144\n.Lfz_b1l:\nv_bitop3_b32 v17, v17, v19, s48 bitop3:0x7e\nv_bitop3_b32 v21, v21, v23, s48 bitop3:0x7e\nv_bitop3_b32 v25, v25, v27, s48 bitop3:0x7e\n" \
  "v_bitop3_b32 v29, v29, v31, s48 bitop3:0x7e\nv_bitop3_b32 v49, v49, v51, s48 bitop3:0x7e\nv_bitop3_b32 v53, v53, v55, s48 bitop3:0x7e\n" \
  "v_bitop3_b32 v57, v57, v59, s48 bitop3:0x7e\nv_bitop3_b32 v61, v61, v63, s48 bitop3:0x7e\nv_xor_b32 v6, s48, v65\nv_or3_b32 v6, v6, v17, v21\n" \
  "v_or3_b32 v7, v25, v29, v49\nv_or3_b32 v8, v53, v57, v61\nv_or3_b32 v6, v6, v7, v8\nv_cmp_ne_u32 vcc, 0, v6\ns_cbranch_vccz .Lfz_b10\n" \
  "global_load_dwordx2 v[64:65], v66, s[24:25] sc1\nglobal_load_dwordx4 v[16:19], v2, s[46:47] offset:-4096 sc1\n" \
  "global_load_dwordx4 v[20:23], v2, s[46:47] offset:-3072 sc1\nglobal_load_dwordx4 v[24:27], v2, s[46:47] offset:-2048 sc1\n" \
  "global_load_dwordx4 v[28:31], v2, s[46:47] offset:-1024 sc1\nglobal_load_dwordx4 v[48:51], v2, s[46:47] offset:0 sc1\n" \
  "global_load_dwordx4 v[52:55], v2, s[46:47] offset:1024 sc1\nglobal_load_dwordx4 v[56:59], v2, s[46:47] offset:2048 sc1\n" \
  "global_load_dwordx4 v[60:63], v2, s[46:47] offset:3072 sc1\ns_waitcnt vmcnt(0)\ns_sub_u32 s37, s37, 1\ns_cbranch_scc0 .Lfz_b1l\n.Lfz_b10:\nv_add_f32 v16, v16, v20\n" \
  "v_add_f32 v18, v18, v22\nv_add_f32 v48, v48, v52\nv_add_f32 v50, v50, v54\nv_add_f32 v16, v16, v24\nv_add_f32 v18, v18, v26\nv_add_f32 v48, v48, v56\n" \
  "v_add_f32 v50, v50, v58\nv_add_f32 v16, v16, v28\nv_add_f32 v18, v18, v30\nv_add_f32 v48, v48, v60\nv_add_f32 v50, v50, v62\nv_mov_b32 v17, v16\nv_mov_b32 v19, v18\n" \
  "v_mov_b32 v49, v48\nv_mov_b32 v51, v50\nv_and_b32 v7, 0xffff0000, v64\nv_lshlrev_b32 v6, 16, v64\nv_permlane32_swap_b32 v16, v17\nv_permlane32_swap_b32 v18, v19\n" \
  "v_permlane32_swap_b32 v48, v49\nv_permlane32_swap_b32 v50, v51\nv_rcp_f32 v7, v7\nv_add_f32 v16, v16, v17\nv_add_f32 v18, v18, v19\nv_add_f32 v48, v48, v49\n" \
  "v_add_f32 v50, v50, v51\nv_fmac_f32 v16, v6, v18\nv_fmac_f32 v48, v6, v50\nv_mul_f32 v16, v16, v7\nv_mul_f32 v48, v48, v7\nv_cvt_pk_bf16_f32 v16, v16, v16\n" \
  "v_and_b32 v16, 0xffff0000, v16\nv_cvt_pk_bf16_f32 v48, v48, v48\nv_and_b32 v48, 0xffff0000, v48\nv_mul_f32 v8, 0xbfb8aa3b, v16\nv_exp_f32 v8, v8\ns_lshl_b32 s40, s27, 6\n" \
  "v_add_f32 v8, 1.0, v8\nv_rcp_f32 v8, v8\ns_add_u32 s40, s40, 155776\nv_mul_f32 v8, v8, v16\nv_cvt_pk_bf16_f32 v8, v8, v8\nv_and_b32 v8, 0xffff0000, v8\n" \
  "v_mul_f32 v8, v8, v48\nv_lshl_add_u32 v9, v1, 1, s40\nv_cvt_pk_bf16_f32 v8, v8, v8\ns_mov_b32 exec_hi, 0\nds_write_b16 v9, v8\ns_mov_b64 exec, -1\ns_branch .Lfz_b12\n" \
  ".Lfz_b14:\nglobal_load_dwordx4 v[88:91], v2, s[60:61] sc1\nglobal_load_dwordx4 v[92:95], v2, s[60:61] offset:1024 sc1\ns_mov_b64 exec, s[50:51]\n" \
  "global_load_dword v72, v85, s[60:61] sc1\ns_mov_b64 exec, -1\n.Lfz_b12:\ns_waitcnt lgkmcnt(0)\ns_barrier\nds_read_b128 v[4:7], v87 offset:0\n" \
  "ds_read_b64_tr_b16 v[128:129], v80 offset:0\nds_read_b64_tr_b16 v[130:131], v80 offset:4096\nds_read_b64_tr_b16 v[132:133], v81 offset:0\n" \
  "ds_read_b64_tr_b16 v[134:135], v81 offset:4096\nds_read_b128 v[8:11], v87 offset:32\nds_read_b64_tr_b16 v[136:137], v80 offset:16384\n" \
  "ds_read_b64_tr_b16 v[138:139], v80 offset:20480\nds_read_b64_tr_b16 v[140:141], v81 offset:16384\nds_read_b64_tr_b16 v[142:143], v81 offset:20480\n" \
  "ds_read_b128 v[64:67], v87 offset:64\nds_read_b64_tr_b16 v[144:145], v80 offset:32768\nds_read_b64_tr_b16 v[146:147], v80 offset:36864\n" \
  "ds_read_b64_tr_b16 v[148:149], v81 offset:32768\nds_read_b64_tr_b16 v[150:151], v81 offset:36864\nds_read_b128 v[68:71], v87 offset:96\n" \
  "ds_read_b64_tr_b16 v[152:153], v80 offset:49152\nds_read_b64_tr_b16 v[154:155], v80 offset:53248\nds_read_b64_tr_b16 v[156:157], v81 offset:49152\n" \
  "ds_read_b64_tr_b16 v[158:159], v81 offset:53248\ns_waitcnt lgkmcnt(10)\nv_mfma_f32_32x32x16_bf16 v[16:31], v[4:7], v[128:131], 0\n" \
  "v_mfma_f32_32x32x16_bf16 v[48:63], v[4:7], v[132:135], 0\nv_mfma_f32_32x32x16_bf16 v[16:31], v[8:11], v[136:139], v[16:31]\n" \
  "v_mfma_f32_32x32x16_bf16 v[48:63], v[8:11], v[140:143], v[48:63]\ns_cmp_lt_u32 s11, 2\ns_cbranch_scc1 .Lfz_b13\ns_waitcnt lgkmcnt(0)\n" \
  "v_mfma_f32_32x32x16_bf16 v[16:31], v[64:67], v[144:147], v[16:31]\nv_mfma_f32_32x32x16_bf16 v[48:63], v[64:67], v[148:151], v[48:63]\n" \
  "v_mfma_f32_32x32x16_bf16 v[16:31], v[68:71], v[152:155], v[16:31]\nv_mfma_f32_32x32x16_bf16 v[48:63], v[68:71], v[156:159], v[48:63]\n.Lfz_b13:\ns_waitcnt lgkmcnt(0)\n" \
  "s_nop 9\ns_mov_b32 exec_lo, 0\nv_mov_b32 v16, v48\ns_mov_b64 exec, -1\nv_mul_f32 v6, v16, v84\ns_nop 1\n" \
  "v_mov_b32_dpp v8, v6 quad_perm:[1,0,3,2] row_mask:0xf bank_mask:0xf\nv_cvt_pk_bf16_f32 v120, v6, v8\ns_nop 1\n" \
  "v_mov_b32_dpp v122, v120 quad_perm:[2,3,0,1] row_mask:0xf bank_mask:0xf\ns_mov_b32 exec_lo, 0x11111111\ns_mov_b32 exec_hi, 0x11111111\n" \
  "global_store_dwordx4 v86, v[120:123], s[24:25] sc1\ns_mov_b64 exec, -1\ns_barrier\ns_cmp_gt_u32 s11, s27\ns_cbranch_scc0 .Lfz_b15\n" \
  "global_load_dwordx4 v[88:91], v2, s[60:61] sc1\nglobal_load_dwordx4 v[92:95], v2, s[60:61] offset:1024 sc1\ns_mov_b64 exec, s[50:51]\n" \
  "global_load_dword v72, v85, s[60:61] sc1\ns_mov_b64 exec, -1\n.Lfz_b15:\nglobal_load_dwordx4 v[96:99], v2, s[62:63] sc1\n" \
  "global_load_dwordx4 v[100:103], v2, s[62:63] offset:1024 sc1\ns_mov_b64 exec, s[50:51]\nglobal_load_dword v73, v85, s[62:63] sc1\ns_mov_b64 exec, -1\ns_branch .Lfz_b1r\n"

__global__ void __launch_bounds__(512, 2) fused(const void* x, const void* wqkv, const void* wo, const void* wr, const void* wg, const void* wu,
                                                const void* wd, void* out, void* ws) {
  asm volatile(FZ_ASM : : "s"(__builtin_amdgcn_kernarg_segment_ptr()), "s"(blockIdx.x), "v"(threadIdx.x) : "v1", "v2", "v3", "v4", "v5", "v6", "v7", "v8", "v9", "v10", "v11", "v12", "v13", "v14", "v15", "v16", "v17", "v18", "v19", "v20", "v21", "v22", "v23", "v24", "v25", "v26", "v27", "v28", "v29", "v30", "v31", "v32", "v33", "v34", "v35", "v36", "v37", "v38", "v39", "v40", "v41", "v42", "v43", "v44", "v45", "v46", "v47", "v48", "v49", "v50", "v51", "v52", "v53", "v54", "v55", "v56", "v57", "v58", "v59", "v60", "v61", "v62", "v63", "v64", "v65", "v66", "v67", "v68", "v69", "v70", "v71", "v72", "v73", "v74", "v75", "v76", "v77", "v78", "v79", "v80", "v81", "v82", "v83", "v84", "v85", "v86", "v87", "v88", "v89", "v90", "v91", "v92", "v93", "v94", "v95", "v96", "v97", "v98", "v99", "v100", "v101", "v102", "v103", "v104", "v105", "v106", "v107", "v108", "v109", "v110", "v111", "v112", "v113", "v114", "v115", "v116", "v117", "v118", "v119", "v120", "v121", "v122", "v123", "v124", "v125", "v126", "v127", "v128", "v129", "v130", "v131", "v132", "v133", "v134", "v135", "v136", "v137", "v138", "v139", "v140", "v141", "v142", "v143", "v144", "v145", "v146", "v147", "v148", "v149", "v150", "v151", "v152", "v153", "v154", "v155", "v156", "v157", "v158", "v159", "v160", "v161", "v162", "v163", "v164", "v165", "v166", "v167", "v168", "v169", "v170", "v171", "v172", "v173", "v174", "v175", "v176", "v177", "v178", "v179", "v180", "v181", "v182", "v183", "v184", "v185", "v186", "v187", "v188", "v189", "v190", "v191", "v192", "v193", "v194", "v195", "v196", "v197", "v198", "v199", "v200", "v201", "v202", "v203", "v204", "v205", "v206", "v207", "v208", "v209", "v210", "v211", "v212", "v213", "v214", "v215", "v216", "v217", "v218", "v219", "v220", "v221", "v222", "v223", "v224", "v225", "v226", "v227", "v228", "v229", "v230", "v231", "v232", "v233", "v234", "v235", "v236", "v237", "v238", "v239", "v240", "v241", "v242", "v243", "v244", "v245", "v246", "v247", "v248", "v249", "v250", "v251", "v252", "v253", "v254", "v255", "s3", "s4", "s5", "s6", "s7", "s8", "s9", "s10", "s11", "s12", "s13", "s14", "s15", "s16", "s17", "s18", "s19", "s20", "s21", "s22", "s23", "s24", "s25", "s26", "s27", "s28", "s29", "s30", "s31", "s35", "s36", "s37", "s38", "s39", "s40", "s41", "s42", "s43", "s44", "s45", "s46", "s47", "s48", "s49", "s50", "s51", "s52", "s53", "s54", "s55", "s56", "s57", "s58", "s59", "s60", "s61", "s62", "s63", "s64", "s65", "s66", "s67", "vcc", "scc", "memory");
}
extern "C" void kernel_launch(void* const* d_in, const int* in_sizes, int n_in, void* d_out, int out_size, void* d_ws, size_t ws_size,
                              hipStream_t stream) {
  (void)hipFuncSetAttribute((const void*)fused, hipFuncAttributeMaxDynamicSharedMemorySize, 163840);
  fused<<<256, 512, 163840, stream>>>(d_in[0], d_in[1], d_in[2], d_in[4], d_in[5], d_in[6], d_in[7], d_out, d_ws);
}
```
